```python
import torch

def _softmax(x, axis=-1):
    return torch.softmax(x, dim=axis)
def _gelu(x):
    return torch.nn.functional.gelu(x, approximate="tanh")
def _silu(x):
    return torch.nn.functional.silu(x)
def _sigmoid(x):
    return torch.sigmoid(x)
def _layernorm(x, axis=-1, eps=1e-5):
    m = x.mean(dim=axis, keepdim=True)
    v = x.var(dim=axis, keepdim=True, unbiased=False)
    return (x - m) / torch.sqrt(v + eps)
def _scatter_add(tgt, idx, src, axis=0):
    return tgt.clone().index_add_(axis, idx, src)
def _segment_sum(src, seg, n):
    out = torch.zeros((n,) + src.shape[1:], dtype=src.dtype)
    return out.index_add_(0, seg, src)
def _pad(x, pw):
    flat = tuple(v for pair in reversed(pw) for v in pair)
    return torch.nn.functional.pad(x, flat)

kernel_name = 'interleave_layers__sliding_block__mixer_channel_mlp__prompt'

def setup_inputs():

    _w = (lambda a: (lambda t: t.to(torch.bfloat16) if t.is_floating_point() else t)(torch.as_tensor(a)))
    def _A_setup():
        B, S, D = 1, 2048, 448
        return {
            "x": _w((lambda *s: torch.randn(*s, dtype=torch.bfloat16) * 0.05)(B, S, D)),
            "wqkv": _w((lambda *s: torch.randn(*s, dtype=torch.bfloat16) * 0.05)(D, 3 * D)),
            "wo": _w((lambda *s: torch.randn(*s, dtype=torch.bfloat16) * 0.05)(D, D)),
        }
    _A_in = _A_setup()

    def _B_setup():
        B, S, D, F = 1, 2048, 448, 1792
        return {
            "x": _w((lambda *s: torch.randn(*s, dtype=torch.bfloat16) * 0.05)(B, S, D)),
            "w1": _w((lambda *s: torch.randn(*s, dtype=torch.bfloat16) * 0.8)(D, F) * (D ** -0.5)),
            "b1": _w((lambda *s: torch.randn(*s, dtype=torch.bfloat16) * 0.8)(F)),
            "w2": _w((lambda *s: torch.randn(*s, dtype=torch.bfloat16) * 0.8)(F, D) * (F ** -0.5)),
            "b2": _w((lambda *s: torch.randn(*s, dtype=torch.bfloat16) * 0.4)(D)),
        }
    _B_in = _B_setup()

    return {
        "A_x": _A_in["x"],
        "A_wqkv": _A_in["wqkv"],
        "A_wo": _A_in["wo"],
        "B_x": _B_in["x"],
        "B_w1": _B_in["w1"],
        "B_b1": _B_in["b1"],
        "B_w2": _B_in["w2"],
        "B_b2": _B_in["b2"],
    }

def reference(A_x, A_wqkv, A_wo, B_x, B_w1, B_b1, B_w2, B_b2):
    def _A_ref(x, wqkv, wo):
        B, S, D = x.shape
        H = 16; Dh = D // H; W = 256
        qkv = (lambda x, *d: x.permute(*d))((x @ wqkv).reshape(B, S, 3, H, Dh), 2, 0, 3, 1, 4)
        q, k, v = qkv[0], qkv[1], qkv[2]
        scores = torch.einsum("bhqd,bhkd->bhqk", q, k) * (Dh ** -0.5)
        idx = torch.arange(S)
        mask = (idx[:, None] - idx[None, :] >= 0) & (idx[:, None] - idx[None, :] < W)
        scores = torch.where(mask[None, None], scores, -1e9)
        p = _softmax(scores, axis=-1)
        o = torch.einsum("bhqk,bhkd->bhqd", p, v)
        return (lambda x, *d: x.permute(*d))(o, 0, 2, 1, 3).reshape(B, S, D) @ wo

    def _B_ref(x, w1, b1, w2, b2):
        y = _layernorm(x, axis=-1)
        h = _gelu(torch.einsum("bsd,df->bsf", y, w1) + b1)
        return torch.einsum("bsf,fd->bsd", h, w2) + b2

    x = A_x
    x = x + _A_ref(_layernorm(x, axis=-1), A_wqkv, A_wo)
    x = x + _B_ref(_layernorm(x, axis=-1), B_w1, B_b1, B_w2, B_b2)
    x = x + _A_ref(_layernorm(x, axis=-1), A_wqkv, A_wo)
    x = x + _B_ref(_layernorm(x, axis=-1), B_w1, B_b1, B_w2, B_b2)
    return x
```

```cpp
#include <hip/hip_runtime.h>
#include <hip/hip_bf16.h>
#include <stdint.h>
namespace nv {
constexpr int S = 2048, D = 448, H = 16, DH = 28, W = 256, F = 1792;
__device__ __forceinline__ float bf2f(unsigned short b) { return __uint_as_float((unsigned)b << 16); }
__device__ __forceinline__ unsigned short f2bf(float f) { unsigned u = __float_as_uint(f); u += 0x7FFFu + ((u >> 16) & 1u); return (unsigned short)(u >> 16); }
__global__ void k_cvt(const unsigned short* in, float* out, int n) { int i = blockIdx.x * 256 + threadIdx.x; if (i < n) out[i] = bf2f(in[i]); }
__global__ void k_out(const float* in, unsigned short* out, int n) { int i = blockIdx.x * 256 + threadIdx.x; if (i < n) out[i] = f2bf(in[i]); }
__global__ void k_ln(const float* x, float* y, int twice) {
  int row = blockIdx.x, l = threadIdx.x; float v[7];
  for (int i = 0; i < 7; ++i) v[i] = x[row * D + l + 64 * i];
  for (int rep = 0; rep <= twice; ++rep) {
    float s = 0; for (int i = 0; i < 7; ++i) s += v[i];
    for (int o = 32; o; o >>= 1) s += __shfl_xor(s, o);
    float m = s / D; float q = 0; for (int i = 0; i < 7; ++i) q += (v[i] - m) * (v[i] - m);
    for (int o = 32; o; o >>= 1) q += __shfl_xor(q, o);
    float r = 1.0f / sqrtf(q / D + 1e-5f);
    for (int i = 0; i < 7; ++i) v[i] = bf2f(f2bf((v[i] - m) * r));
  }
  for (int i = 0; i < 7; ++i) y[row * D + l + 64 * i] = v[i];
}
__global__ void k_gemm(const float* A, const unsigned short* B, const unsigned short* bias, float* C, int N, int K, int mode) {
  __shared__ float sa[16][17], sb[16][17];
  int tx = threadIdx.x & 15, ty = threadIdx.x >> 4; int row = blockIdx.y * 16 + ty, col = blockIdx.x * 16 + tx; float acc = 0;
  for (int k0 = 0; k0 < K; k0 += 16) {
    sa[ty][tx] = A[row * K + k0 + tx]; sb[ty][tx] = bf2f(B[(k0 + ty) * N + col]);
    __syncthreads();
    for (int k = 0; k < 16; ++k) acc += sa[ty][k] * sb[k][tx];
    __syncthreads();
  }
  if (mode == 0) C[row * N + col] = bf2f(f2bf(acc));
  else if (mode == 1) { float u = acc + bf2f(bias[col]); float t = tanhf(0.7978845608028654f * (u + 0.044715f * u * u * u)); C[row * N + col] = bf2f(f2bf(0.5f * u * (1.f + t))); }
  else if (mode == 2) C[row * N + col] = bf2f(f2bf(C[row * N + col] + acc));
  else C[row * N + col] = bf2f(f2bf(C[row * N + col] + acc + bf2f(bias[col])));
}
__global__ void k_attn(const float* qkv, float* o) {
  int q = blockIdx.x, h = blockIdx.y, l = threadIdx.x; float s[4]; float m = -1e30f;
  for (int i = 0; i < 4; ++i) { int k = q - (l + 64 * i); float a = -1e30f;
    if (k >= 0) { a = 0; for (int d = 0; d < DH; ++d) a += qkv[q * 3 * D + h * DH + d] * qkv[k * 3 * D + D + h * DH + d]; a *= rsqrtf((float)DH); }
    s[i] = a; m = fmaxf(m, a); }
  for (int of = 32; of; of >>= 1) m = fmaxf(m, __shfl_xor(m, of));
  float sum = 0; for (int i = 0; i < 4; ++i) { s[i] = (q - (l + 64 * i) >= 0) ? __expf(s[i] - m) : 0.f; sum += s[i]; }
  for (int of = 32; of; of >>= 1) sum += __shfl_xor(sum, of);
  for (int d = 0; d < DH; ++d) { float a = 0;
    for (int i = 0; i < 4; ++i) { int k = q - (l + 64 * i); if (k >= 0) a += s[i] * qkv[k * 3 * D + 2 * D + h * DH + d]; }
    for (int of = 32; of; of >>= 1) a += __shfl_xor(a, of);
    if (l == 0) o[q * D + h * DH + d] = bf2f(f2bf(a / sum)); }
}
static void run(void* const* d_in, void* d_out, void* d_ws, hipStream_t st) {
  const unsigned short *x = (const unsigned short*)d_in[0], *wqkv = (const unsigned short*)d_in[1], *wo = (const unsigned short*)d_in[2],
    *w1 = (const unsigned short*)d_in[4], *b1 = (const unsigned short*)d_in[5], *w2 = (const unsigned short*)d_in[6], *b2 = (const unsigned short*)d_in[7];
  float* X = (float*)d_ws; float* Y = X + S * D; float* QKV = Y + S * D; float* O = QKV + S * 3 * D; float* Hh = O + S * D;
  k_cvt<<<(S * D + 255) / 256, 256, 0, st>>>(x, X, S * D);
  for (int layer = 0; layer < 2; ++layer) {
    k_ln<<<S, 64, 0, st>>>(X, Y, 0);
    k_gemm<<<dim3(3 * D / 16, S / 16), 256, 0, st>>>(Y, wqkv, nullptr, QKV, 3 * D, D, 0);
    k_attn<<<dim3(S, H), 64, 0, st>>>(QKV, O);
    k_gemm<<<dim3(D / 16, S / 16), 256, 0, st>>>(O, wo, nullptr, X, D, D, 2);
    k_ln<<<S, 64, 0, st>>>(X, Y, 1);
    k_gemm<<<dim3(F / 16, S / 16), 256, 0, st>>>(Y, w1, b1, Hh, F, D, 1);
    k_gemm<<<dim3(D / 16, S / 16), 256, 0, st>>>(Hh, w2, b2, X, D, F, 3);
  }
  k_out<<<(S * D + 255) / 256, 256, 0, st>>>(X, (unsigned short*)d_out, S * D);
}
}
extern "C" void kernel_launch(void* const* d_in, const int* in_sizes, int n_in, void* d_out, int out_size, void* d_ws, size_t ws_size, hipStream_t stream) {
  nv::run(d_in, d_out, d_ws, stream);
}
```

```cpp
#include <hip/hip_runtime.h>
#include <stdint.h>
#pragma clang diagnostic ignored "-Winline-asm"

namespace mk {
typedef unsigned v4u __attribute__((ext_vector_type(4)));
typedef unsigned v2u __attribute__((ext_vector_type(2)));
typedef float f32x4 __attribute__((ext_vector_type(4)));
typedef float f32x2 __attribute__((ext_vector_type(2)));
typedef float f32x16 __attribute__((ext_vector_type(16)));
typedef short s16x4 __attribute__((ext_vector_type(4)));
typedef __bf16 bf16x8 __attribute__((ext_vector_type(8)));
typedef __bf16 bf16x2 __attribute__((ext_vector_type(2)));
typedef unsigned long long u64;
typedef __amdgpu_buffer_rsrc_t rsrc_t;
#define LAS __attribute__((address_space(3)))
#define GAS __attribute__((address_space(1)))
#define RLX __ATOMIC_RELAXED, __HIP_MEMORY_SCOPE_AGENT
#define DI __device__ __forceinline__
#define SB() __builtin_amdgcn_sched_barrier(0)

constexpr int SEQ = 2048, DM = 448, FF = 1792;
constexpr int U = 28672;
constexpr int L_K = 3 * U, L_V = L_K + 22 * 1024, L_WO = L_K + 46 * 1024, L_MISC = L_WO + U, L_TOTAL = 163840;
constexpr int L_CS1 = 4 * U;
constexpr int L_RED = 2 * U, L_RST = L_RED + 65536;
constexpr int M_CSQ = L_MISC, M_B1 = L_MISC + 512, M_ST = L_MISC + 1024;
constexpr int L_XS = 2 * U;
static_assert(L_MISC + 2048 == L_TOTAL && L_XS + 4 * 24576 <= L_MISC && L_RST + 4096 <= L_WO, "LDS map");
constexpr int W_E = 0, W_STARTED = 4096, W_FLAG = 8192;
enum { E_KV = 0, E_O = 1, E_X1 = 2, E_H0 = 3, E_H1 = 4, E_X2 = 5, E_W2 = 6, NEDGE = 8 };
constexpr int W_KV = 1 << 20, KV_L = 16 * 64 * 4096;
constexpr int XB = 32 * 1024, X_L = 64 * XB;
constexpr int X_ST = 28 * 1024;
constexpr int W_O = W_KV + 2 * KV_L, W_X1 = W_O + 2 * X_L, W_X2 = W_X1 + 2 * X_L, W_H = W_X2 + X_L;
constexpr int HB = 112 * 1024, H_L = 64 * HB;
constexpr int W_W2T = W_H + 2 * H_L;
constexpr int W_END = W_W2T + 7 * 112 * 2048;
constexpr int W_STAMP = W_END;
constexpr int SPIN_CAP = 1 << 16;
constexpr unsigned ONES = 0x3f803f80u;

DI f32x16 mfma(v4u a, v4u b, f32x16 c) {
  return __builtin_amdgcn_mfma_f32_32x32x16_bf16(__builtin_bit_cast(bf16x8, a), __builtin_bit_cast(bf16x8, b), c, 0, 0, 0);
}
DI void mfma_v0(f32x16& acc, v4u a, v4u b) { asm volatile("v_mfma_f32_32x32x16_bf16 %0, %1, %2, 0" : "=&v"(acc) : "v"(a), "v"(b)); }
DI void mfma_vv(f32x16& acc, v4u a, v4u b) { asm volatile("v_mfma_f32_32x32x16_bf16 %0, %1, %2, %0" : "+v"(acc) : "v"(a), "v"(b)); }
DI void mfma_a0(f32x16& acc, v4u a, v4u b) { asm volatile("v_mfma_f32_32x32x16_bf16 %0, %1, %2, 0" : "=&v"(acc) : "a"(a), "v"(b)); }
DI void mfma_av(f32x16& acc, v4u a, v4u b) { asm volatile("v_mfma_f32_32x32x16_bf16 %0, %1, %2, %0" : "+v"(acc) : "a"(a), "v"(b)); }
#define MFMA_DONE(x) asm volatile("s_nop 11" : "+v"(x))
DI v2u trd(LAS unsigned char* L, unsigned a) {
  return __builtin_bit_cast(v2u, __builtin_amdgcn_ds_read_tr16_b64_v4i16((LAS s16x4*)(L + a)));
}
DI v4u trf(LAS unsigned char* L, unsigned a0, unsigned a1) { const v2u p = trd(L, a0), q = trd(L, a1); return v4u{p.x, p.y, q.x, q.y}; }
DI unsigned pk(float a, float b) { f32x2 f = {a, b}; return __builtin_bit_cast(unsigned, __builtin_convertvector(f, bf16x2)); }
DI unsigned pk(f32x2 f) { return __builtin_bit_cast(unsigned, __builtin_convertvector(f, bf16x2)); }
DI float blo(unsigned u) { return __builtin_bit_cast(float, u << 16); }
DI float bhi(unsigned u) { return __builtin_bit_cast(float, u & 0xffff0000u); }
DI f32x2 unpk(unsigned u) { return f32x2{blo(u), bhi(u)}; }
DI int rfl(int x) { return __builtin_amdgcn_readfirstlane(x); }
DI float dot2(unsigned a, unsigned b, float c) { return __builtin_amdgcn_fdot2_f32_bf16(__builtin_bit_cast(bf16x2, a), __builtin_bit_cast(bf16x2, b), c, false); }
DI float max3(float a, float b, float c) { float r; asm("v_max3_f32 %0, %1, %2, %3" : "=v"(r) : "v"(a), "v"(b), "v"(c)); return r; }
DI float xh(float v) {
  const unsigned u = __builtin_bit_cast(unsigned, v);
  const auto r = __builtin_amdgcn_permlane32_swap(u, u, false, false);
  return __builtin_bit_cast(float, (threadIdx.x & 32) ? r[0] : r[1]);
}
DI rsrc_t mkrs(const void* p, int bytes) { return __builtin_amdgcn_make_buffer_rsrc((void*)p, 0, bytes, 0x00020000); }
DI void dma16(rsrc_t rs, unsigned voff, unsigned soff, unsigned ldst) {
  asm volatile("s_mov_b32 m0, %0\n\ts_nop 0\n\tbuffer_load_dwordx4 %1, %2, %3 offen lds" :: "s"(ldst), "v"(voff), "s"(rs), "s"(soff) : "memory");
}
#define VMWAIT(n) asm volatile("s_waitcnt vmcnt(" #n ")" ::: "memory")
DI unsigned tr_base(int lane, int tp, bool nat, int par) {
  const int h = lane >> 5, cb = (lane >> 4) & 1, q = (lane >> 2) & 3, p = lane & 3;
  const int row = nat ? 8 * h + 4 * tp + q : 8 * tp + 4 * h + q;
  return row * 64 + ((4 * cb + p + par) & 7) * 8;
}
DI unsigned tr_w1(int lane, int tp, int cc) {
  const int h = lane >> 5, cb = (lane >> 4) & 1, q = (lane >> 2) & 3, p = lane & 3;
  return (8 * tp + 4 * h + q) * 256 + ((cc ^ q) * 64) + (4 * cb + p) * 8;
}
DI void stage_img(rsrc_t rs, int lane, int stride, int coloff, unsigned ldst, int w, int rot) {
  const unsigned vo = (lane >> 2) * stride + (lane & 3) * 16;
  const int wv = (w + 2 * rot) & 3;
#pragma unroll 1
  for (int i = 7 * wv; i < 7 * wv + 7; ++i) dma16(rs, vo, i * 16 * stride + coloff, ldst + i * 1024);
}
DI unsigned w1_voff(int lane) { return (lane >> 4) * 3584 + (((lane & 15) ^ ((lane >> 4) << 2)) * 16); }
DI void stage_w1(rsrc_t rs, int lane, int j, int first, int cnt) {
  const unsigned vo = w1_voff(lane);
#pragma unroll 1
  for (int i = first; i < first + cnt; ++i) dma16(rs, vo, i * 4 * 3584 + j * 256, i * 1024);
}
template <int N> DI void xwait(v4u& x) { asm volatile("s_waitcnt vmcnt(%1)" : "+v"(x) : "i"(N) : "memory"); }
template <int I> DI void xload(v4u& x, rsrc_t rs, unsigned voff) {
  asm volatile("buffer_load_dwordx4 %0, %1, %2, %3 offen offset:%4 sc1" : "=v"(x) : "v"(voff), "s"(rs), "s"((I >> 2) * 4096), "i"((I & 3) * 1024) : "memory");
}
template <int I> DI void w2load(v4u (&w2f)[28][2], rsrc_t rs, unsigned voff) {
  asm volatile("buffer_load_dwordx4 %0, %1, %2, %3 offen offset:%4 sc1" : "=a"(w2f[I >> 1][I & 1]) : "v"(voff), "s"(rs), "s"((I >> 2) * 4096), "i"((I & 3) * 1024) : "memory");
}
template <int LO, int HI> DI void w2range(v4u (&w2f)[28][2], rsrc_t rs, unsigned voff) {
  if constexpr (LO < HI) { w2load<LO>(w2f, rs, voff); w2range<LO + 1, HI>(w2f, rs, voff); }
}
constexpr int LA = 8;
template <int NC> struct KP { static constexpr int PD = NC >= 4 ? 1 : (NC == 2 ? 2 : 4); };
struct NoBg { template <int S> DI void at() const {} };
template <int NC, bool LOAD, int KS, int S, class BG>
DI void kstep(LAS unsigned char* L, v4u (&xf)[28], rsrc_t rs, unsigned voff, unsigned (&b0)[NC], unsigned (&b1)[NC], v4u (&wb)[KP<NC>::PD + 1][NC], f32x16 (&acc)[NC], const BG& bg) {
  constexpr int PD = KP<NC>::PD;
  if constexpr (S < 28) {
    constexpr int SN = S + PD, HI = SN * KS >= 65536 - KS ? 14 * KS : 0;
    if constexpr (LOAD) xwait<(S + LA <= 28 ? LA - 1 : 27 - S)>(xf[S]);
    SB();
#pragma unroll
    for (int q = 0; q < NC; ++q) {
      if (S == 0) mfma_v0(acc[q], wb[0][q], xf[0]); else mfma_vv(acc[q], wb[S % (PD + 1)][q], xf[S]);
      SB();
      if constexpr (SN < 28) {
        if constexpr (HI != 0 && (SN - 1) * KS < 65536 - KS) { b0[q] += HI; b1[q] += HI; }
        wb[SN % (PD + 1)][q] = trf(L, b0[q] + SN * KS - HI, b1[q] + SN * KS - HI);
      }
      if (q == 0) { if constexpr (LOAD && S + LA < 28) xload<S + LA>(xf[S + LA], rs, voff); bg.template at<S>(); }
      SB();
    }
    kstep<NC, LOAD, KS, S + 1, BG>(L, xf, rs, voff, b0, b1, wb, acc, bg);
  }
}
template <int I> DI void xfirst(v4u (&xf)[28], rsrc_t rs, unsigned voff) { if constexpr (I < LA) { xload<I>(xf[I], rs, voff); xfirst<I + 1>(xf, rs, voff); } }
template <int NC, bool LOAD, int KS, class BG>
DI void kloop(LAS unsigned char* L, v4u (&xf)[28], rsrc_t rs, unsigned voff, unsigned (&b0)[NC], unsigned (&b1)[NC], f32x16 (&acc)[NC], const BG& bg) {
  constexpr int PD = KP<NC>::PD;
  v4u wb[PD + 1][NC];
  if constexpr (LOAD) xfirst<0>(xf, rs, voff);
#pragma unroll
  for (int s = 0; s < PD; ++s)
#pragma unroll
    for (int q = 0; q < NC; ++q) wb[s][q] = trf(L, b0[q] + s * KS, b1[q] + s * KS);
  kstep<NC, LOAD, KS, 0, BG>(L, xf, rs, voff, b0, b1, wb, acc, bg);
}
struct BgW1 {
  rsrc_t rs; unsigned vo, so, ld;
  template <int S> DI void at() const { if constexpr ((S & 1) == 0) dma16(rs, vo, so + (S >> 1) * 4 * 3584, ld + (S >> 1) * 1024); }
};
template <int KS> DI void colsum(LAS unsigned char* L, unsigned b0, unsigned b1, unsigned dst, int lane) {
  const v4u ones = {ONES, ONES, ONES, ONES};
  f32x16 a;
#pragma unroll
  for (int r = 0; r < 16; ++r) a[r] = 0.f;
#pragma unroll 1
  for (int s = 0; s < 28; s += 7) {
    v4u f[7];
#pragma unroll
    for (int i = 0; i < 7; ++i) f[i] = trf(L, b0 + (s + i) * KS, b1 + (s + i) * KS);
#pragma unroll
    for (int i = 0; i < 7; ++i) a = mfma(f[i], ones, a);
  }
  if ((lane & 31) == 0) {
#pragma unroll
    for (int q = 0; q < 4; ++q) *(LAS f32x4*)(L + dst + q * 32 + (lane >> 5) * 16) = f32x4{a[4 * q], a[4 * q + 1], a[4 * q + 2], a[4 * q + 3]};
  }
}
DI GAS u64* flag_line(unsigned char* ws, int layer, int edge, int wg) { return (GAS u64*)(ws + W_FLAG + ((layer * 256 + wg) * NEDGE + edge) * 128); }
DI void poll(GAS u64* p, u64 epoch, bool& dead) {
  if (dead) return;
  for (int spins = 0;; ++spins) {
    const u64 f = __hip_atomic_load(p, RLX);
    if (__builtin_amdgcn_ballot_w64(f != epoch) == 0) break;
    if (spins > SPIN_CAP) { dead = true; break; }
  }
  __builtin_amdgcn_fence(__ATOMIC_ACQUIRE, "wavefront");
}
#define STAMP(i)
#define STAMPC(i)
DI void put_k(LAS unsigned char* L, int lt, int lane, v4u a, v4u b) {
  *(LAS v4u*)(L + L_K + lt * 2048 + lane * 16) = a;
  *(LAS v4u*)(L + L_K + lt * 2048 + 1024 + lane * 16) = b;
}
DI void put_v(LAS unsigned char* L, int lt, int lane, v4u a, v4u b) {
  const unsigned o = L_V + lt * 2048 + (lane & 31) * 64 + (lane >> 5) * 8;
  *(LAS v2u*)(L + o) = v2u{a.x, a.y};
  *(LAS v2u*)(L + o + 16) = v2u{a.z, a.w};
  *(LAS v2u*)(L + o + 32) = v2u{b.x, b.y};
  *(LAS v2u*)(L + o + 48) = v2u{b.z, b.w};
}
DI void ln_pack(LAS unsigned char* L, const f32x16& a, unsigned cs, int h, float nmsc, float sc, float pad0, v4u& o0, v4u& o1) {
  unsigned o[8];
  f32x4 cq[4];
  f32x2 t[8];
#pragma unroll
  for (int q = 0; q < 4; ++q) cq[q] = *(LAS f32x4*)(L + cs + q * 32 + h * 16);
#pragma unroll
  for (int e = 0; e < 8; ++e) t[e] = f32x2{a[2 * e], a[2 * e + 1]} * sc;
  SB();
#pragma unroll
  for (int e = 0; e < 8; ++e) t[e] += f32x2{cq[e >> 1][2 * (e & 1)], cq[e >> 1][2 * (e & 1) + 1]} * nmsc;
  SB();
#pragma unroll
  for (int e = 0; e < 8; ++e) o[e] = pk(t[e]);
  if (h == 1) { o[6] = pk(pad0, 0.f); o[7] = 0u; }
  o0 = v4u{o[0], o[1], o[2], o[3]}; o1 = v4u{o[4], o[5], o[6], o[7]};
}

__global__ void __launch_bounds__(256, 1) fused(const unsigned short* __restrict__ xin, const unsigned short* __restrict__ wqkv, const unsigned short* __restrict__ wo,
    const unsigned short* __restrict__ w1, const unsigned short* __restrict__ b1, const unsigned short* __restrict__ w2, const unsigned short* __restrict__ b2,
    unsigned short* __restrict__ out, unsigned char* ws) {
  extern __shared__ __attribute__((aligned(16))) unsigned char smem[];
  LAS unsigned char* L = (LAS unsigned char*)smem;
  int lane = threadIdx.x & 63, w = rfl(threadIdx.x >> 6), n = lane & 31, h = lane >> 5;
  int c = 2 * (blockIdx.x & 7) + (blockIdx.x >> 7), j = (blockIdx.x >> 3) & 15;
  const bool mlp = j < 14;
  const int c0_ = c, j0_ = j, w0_ = w, lane0_ = lane;
#define wg (c * 16 + j)
#define TT (4 * c + w)
#define hf5 (j >= 7 ? 1 : 0)
#define jb (j >= 7 ? j - 7 : j)
#define QCOL ((56 * j) & ~15)
#define W2V (W_W2T + (jb * 112 + 28 * w) * 2048 + lane * 16)
#define W2UNIT (7 * c + 4 * hf5 + w)
#define FRESH() do { c = c0_; j = j0_; w = w0_; lane = lane0_; asm volatile("" : "+s"(c), "+s"(j), "+s"(w)); asm volatile("" : "+v"(lane)); n = lane & 31; h = lane >> 5; } while (0)
  const rsrc_t rws = mkrs(ws, W_END), rx = mkrs(xin, SEQ * DM * 2), rqkv = mkrs(wqkv, DM * 3 * DM * 2), rwo = mkrs(wo, mlp ? DM * DM * 2 : 0),
               rw1 = mkrs(w1, mlp ? DM * FF * 2 : 0), rw2 = mkrs(w2, FF * DM * 2), rout = mkrs(out, SEQ * DM * 2), rb2 = mkrs(b2, DM * 2);
  int layer = 0;
  STAMP(0);
  const u64 epoch = *(const u64*)(ws + W_E) + 1;
  bool dead = false;
  v4u xf[28];
  v4u w2f[28][2];
#pragma unroll
  for (int i = 0; i < 28; ++i) { asm volatile("" : "=a"(w2f[i][0])); asm volatile("" : "=a"(w2f[i][1])); }

  for (; layer < 2; ++layer) {
    FRESH();
    float mean, var;
    f32x16 akv[2];
    const unsigned tq0 = tr_base(lane, 0, true, j & 1), tq1 = tr_base(lane, 1, true, j & 1);
    unsigned ba[2] = {tq0, tq0 + U}, bb[2] = {tq1, tq1 + U};
    if (layer == 0) {
      stage_img(rqkv, lane, 2688, 896 + QCOL, 0, w, c & 1);
      stage_img(rqkv, lane, 2688, 1792 + QCOL, U, w, c & 1);
      {
        const unsigned vo = (32 * TT + (lane >> 2)) * 896 + (((lane & 3) ^ ((lane >> 4) & 3)) * 16);
        int cb = j >= 12 ? j - 12 : j;
#pragma unroll 1
        for (int i = 0; i < 12; ++i) {
          dma16(rx, vo, cb * 64, L_XS + w * 24576 + cb * 2048);
          dma16(rx, vo, cb * 64 + 16 * 896, L_XS + w * 24576 + cb * 2048 + 1024);
          cb = cb == 11 ? 0 : cb + 1;
        }
#pragma unroll
        for (int i = 24; i < 28; ++i) xf[i] = __builtin_amdgcn_raw_buffer_load_b128(rx, (32 * TT + n) * 896 + 16 * h + 32 * i, 0, 0);
      }
      STAMP(22);
      VMWAIT(28);
      __syncthreads();
      STAMP(17);
      if (w < 2) colsum<1024>(L, tq0 + w * U, tq1 + w * U, M_CSQ + w * 128, lane);
      if (threadIdx.x == 128) __hip_atomic_store((GAS u64*)(ws + W_STARTED) + wg, epoch, RLX);
      if (w == 3 && mlp) {
        const unsigned bv = *(const unsigned*)(b1 + 128 * j + 2 * lane);
        *(LAS f32x2*)(L + M_B1 + lane * 8) = unpk(bv);
      }
      VMWAIT(0);
      asm volatile("" : "+v"(xf[24]), "+v"(xf[25]), "+v"(xf[26]), "+v"(xf[27]));
      STAMP(18);
      const unsigned xb = L_XS + w * 24576 + (n >> 4) * 1024 + (n & 15) * 64;
      const unsigned xe = xb + ((h ^ ((n >> 2) & 3)) * 16), xo = xb + (((2 + h) ^ ((n >> 2) & 3)) * 16);
#pragma unroll
      for (int i = 0; i < 24; ++i) xf[i] = *(LAS v4u*)(L + ((i & 1) ? xo : xe) + (i >> 1) * 2048);
      __syncthreads();
      {
        const int so = (mlp && w + hf5 < 4) ? W2UNIT * 16 * 896 + jb * 128 : FF * DM * 2;
        dma16(rw2, (lane >> 3) * 896 + (lane & 7) * 16, so, L_K + w * 2048);
        dma16(rw2, (lane >> 3) * 896 + (lane & 7) * 16, so + 8 * 896, L_K + w * 2048 + 1024);
      }
      stage_img(rqkv, lane, 2688, QCOL, 2 * U, w, c & 1);
      STAMP(23);
      float s[2] = {0.f, 0.f}, q[2] = {0.f, 0.f};
#pragma unroll
      for (int i = 0; i < 28; ++i)
#pragma unroll
        for (int e = 0; e < 4; ++e) { s[e & 1] = dot2(xf[i][e], ONES, s[e & 1]); q[e & 1] = dot2(xf[i][e], xf[i][e], q[e & 1]); }
      mean = s[0] + s[1]; var = q[0] + q[1];
      stage_img(rwo, lane, 896, j * 64, L_WO, w, c & 1);
      STAMP(1);
      kloop<2, false, 1024>(L, xf, rws, 0u, ba, bb, akv, NoBg{});
    } else {
      v2u st[4];
#pragma unroll
      for (int i = 0; i < 4; ++i) st[i] = __builtin_amdgcn_raw_buffer_load_b64(rws, (4 * h + i == 7) ? W_END : W_X2 + TT * XB + X_ST + (4 * h + i) * 256 + n * 8, 0, 16);
      SB();
      STAMP(1);
      kloop<2, true, 1024>(L, xf, rws, W_X2 + TT * XB + lane * 16, ba, bb, akv, NoBg{});
      mean = 0.f; var = 0.f;
#pragma unroll
      for (int i = 0; i < 4; ++i) { const f32x2 f = __builtin_bit_cast(f32x2, st[i]); mean += f[0]; var += f[1]; }
    }
    mean += xh(mean); var += xh(var);
    mean *= (1.0f / DM);
    var = fmaxf(var * (1.0f / DM) - mean * mean, 0.f);
    float rs = __builtin_amdgcn_rsqf(var + 1e-5f);
    v4u qf[2], kf[2];
    {
      asm volatile("s_nop 11" : "+v"(akv[0]), "+v"(akv[1]));
      const int kvoff = W_KV + layer * KV_L + (j * 64 + TT) * 4096 + lane * 16;
      v4u v0, v1;
      ln_pack(L, akv[0], M_CSQ, h, -mean * rs, rs, 0.f, kf[0], kf[1]);
      __builtin_amdgcn_raw_buffer_store_b128(kf[0], rws, kvoff, 0, 16);
      __builtin_amdgcn_raw_buffer_store_b128(kf[1], rws, kvoff + 1024, 0, 16);
      ln_pack(L, akv[1], M_CSQ + 128, h, -mean * rs, rs, 1.f, v0, v1);
      __builtin_amdgcn_raw_buffer_store_b128(v0, rws, kvoff + 2048, 0, 16);
      __builtin_amdgcn_raw_buffer_store_b128(v1, rws, kvoff + 3072, 0, 16);
      STAMP(19);
      if (layer == 0) {
        VMWAIT(11);
        const unsigned ta = L_K + w * 2048 + (4 * h + ((lane >> 2) & 3)) * 128 + (8 * ((lane >> 4) & 1) + 4 * ((lane >> 1) & 1) + 2 * (lane & 1)) * 8;
        const int wo_ = (mlp && w + hf5 < 4) ? W_W2T + (jb * 112 + W2UNIT) * 2048 + lane * 16 : W_END;
        __builtin_amdgcn_raw_buffer_store_b128(trf(L, ta, ta + 1024), rws, wo_, 0, 16);
        __builtin_amdgcn_raw_buffer_store_b128(trf(L, ta + 8, ta + 1032), rws, wo_ + 1024, 0, 16);
      }
      __syncthreads();
      if (layer == 0 && w == 2) colsum<1024>(L, tq0 + 2 * U, tq1 + 2 * U, M_CSQ + 256, lane);
      if (w < 3) put_k(L, 8 + w, lane, kf[0], kf[1]);
      put_v(L, 8 + w, lane, v0, v1);
      f32x16 aq[1];
      unsigned bq0[1] = {tq0 + 2 * U}, bq1[1] = {tq1 + 2 * U};
      kloop<1, false, 1024>(L, xf, rws, 0u, bq0, bq1, aq, BgW1{rw1, w1_voff(lane), (unsigned)(14 * w * 4 * 3584 + j * 256), (unsigned)(14 * w * 1024)});
      VMWAIT(14);
      __syncthreads();
      STAMP(2);
      FRESH();
      if (w == 0 && lane < 2 && c + 1 + lane < 16) __hip_atomic_store(flag_line(ws, layer, E_KV, wg + 16 * (1 + lane)) + lane, epoch, RLX);
      if (layer == 0 && mlp && w == 1 && lane < 32) __hip_atomic_store(flag_line(ws, 0, E_W2, (lane >> 1) * 16 + jb + 7 * (lane & 1)) + 2 * c + hf5, epoch, RLX);
      stage_w1(rw1, lane, j, 56 + 7 * w, 7);
      MFMA_DONE(aq[0]);
      const float sq = rs * (0.18898223650461362f * 1.4426950408889634f);
      ln_pack(L, aq[0], M_CSQ + 256, h, -mean * sq, sq, 0.f, qf[0], qf[1]);
    }
    f32x16 sc[9];
#pragma unroll
    for (int r = 0; r < 16; ++r) {
      const bool past = (r & 3) + 8 * (r >> 2) + 4 * h > n;
      sc[0][r] = past ? 0.f : -1e30f;
      sc[8][r] = past ? -1e30f : 0.f;
    }
    asm volatile("s_nop 1" : "+v"(sc[0]), "+v"(sc[8]), "+v"(qf[0]), "+v"(qf[1]));
    mfma_vv(sc[8], kf[0], qf[0]); mfma_vv(sc[8], kf[1], qf[1]);
    if (w == 0 && c > 0) poll(flag_line(ws, layer, E_KV, wg) + (lane < (c > 1 ? 2 : 1) ? lane : 0), epoch, dead);
    __syncthreads();
    STAMP(3);
    FRESH();
    const unsigned tp0 = tr_base(lane, 0, false, 0), tp1 = tr_base(lane, 1, false, 0);
    {
      v4u hk[2][4];
#pragma unroll
      for (int hh = 0; hh < 2; ++hh) {
        const int Tg = 4 * c - 8 + 2 * w + hh;
        const int o = Tg >= 0 ? W_KV + layer * KV_L + (j * 64 + Tg) * 4096 : W_END;
#pragma unroll
        for (int e = 0; e < 4; ++e) hk[hh][e] = __builtin_amdgcn_raw_buffer_load_b128(rws, lane * 16 + e * 1024, o, 16);
      }
      float m0 = -1e30f, m1 = -1e30f;
#define TMAX(T, a, b) do { _Pragma("unroll") for (int r_ = a; r_ < b; r_ += 4) { m0 = max3(m0, T[r_], T[r_ + 1]); m1 = max3(m1, T[r_ + 2], T[r_ + 3]); } } while (0)
      MFMA_DONE(sc[8]);
      TMAX(sc[8], 0, 16);
      SB();
#pragma unroll
      for (int hh = 0; hh < 2; ++hh) {
        put_k(L, 2 * w + hh, lane, hk[hh][0], hk[hh][1]);
        put_v(L, 2 * w + hh, lane, hk[hh][2], hk[hh][3]);
      }
      __syncthreads();
      STAMP(4); STAMPC(27);
      v4u kb[3][2];
#define KRD(i) do { kb[(i) % 3][0] = *(LAS v4u*)(L + L_K + (w + (i)) * 2048 + lane * 16); kb[(i) % 3][1] = *(LAS v4u*)(L + L_K + (w + (i)) * 2048 + 1024 + lane * 16); } while (0)
#define CLIP(i) do { if (__builtin_expect(TT - 8 + (i) < 0, 0)) { asm volatile("" ::: "memory"); _Pragma("unroll") for (int r_ = 0; r_ < 16; ++r_) sc[i][r_] = -1e30f; } } while (0)
      KRD(0); KRD(1); KRD(2);
      SB();
      mfma_vv(sc[0], kb[0][0], qf[0]); mfma_vv(sc[0], kb[0][1], qf[1]);
#pragma unroll
      for (int i = 1; i < 8; ++i) {
        mfma_v0(sc[i], kb[i % 3][0], qf[0]);
        asm volatile("s_nop 3" : "+v"(sc[i - 1]));
        CLIP(i - 1);
        TMAX(sc[i - 1], 0, 8);
        SB();
        mfma_vv(sc[i], kb[i % 3][1], qf[1]);
        if (i + 2 < 8) KRD(i + 2);
        TMAX(sc[i - 1], 8, 16);
        SB();
      }
      const unsigned vb = L_V + w * 2048;
      v4u vf[3];
      vf[0] = trf(L, tp0 + vb, tp1 + vb);
      vf[1] = trf(L, tp0 + vb + 1024, tp1 + vb + 1024);
      MFMA_DONE(sc[7]);
      CLIP(7);
      TMAX(sc[7], 0, 16);
      float m = max3(m0, m1, m1);
      m = max3(m, xh(m), m);
      const f32x2 nm = {-m, -m};
      f32x16 o;
#pragma unroll
      for (int r = 0; r < 16; ++r) o[r] = 0.f;
#pragma unroll
      for (int st = 0; st < 18; ++st) {
        f32x2 e[4];
#pragma unroll
        for (int d = 0; d < 4; ++d) e[d] = f32x2{sc[st >> 1][8 * (st & 1) + 2 * d], sc[st >> 1][8 * (st & 1) + 2 * d + 1]} + nm;
        v4u pf;
        SB();
#pragma unroll
        for (int d = 0; d < 4; ++d) { e[d][0] = __builtin_amdgcn_exp2f(e[d][0]); e[d][1] = __builtin_amdgcn_exp2f(e[d][1]); }
        SB();
#pragma unroll
        for (int d = 0; d < 4; ++d) pf[d] = pk(e[d]);
        SB();
        if (st + 2 < 18) vf[(st + 2) % 3] = trf(L, tp0 + vb + (st + 2) * 1024, tp1 + vb + (st + 2) * 1024);
        SB();
        o = mfma(vf[st % 3], pf, o);
        SB();
      }
      const float other = xh(o[12]);
      const float inv = __builtin_amdgcn_rcpf(h ? o[12] : other);
      const int ob = W_O + layer * X_L + TT * XB + n * 16;
#pragma unroll
      for (int q = 0; q < 4; ++q) {
        const int P = 7 * j + 2 * q + h;
        const int off = (q == 3 && h == 1) ? W_END : ob + (P >> 2) * 1024 + (P & 1) * 512 + ((P >> 1) & 1) * 8;
        __builtin_amdgcn_raw_buffer_store_b64(v2u{pk(o[4 * q] * inv, o[4 * q + 1] * inv), pk(o[4 * q + 2] * inv, o[4 * q + 3] * inv)}, rws, off, 0, 16);
      }
      STAMP(20); STAMPC(28);
      VMWAIT(0);
      __syncthreads();
      STAMP(5);
      if (w == 0 && lane < 14) __hip_atomic_store(flag_line(ws, layer, E_O, c * 16 + lane) + j, epoch, RLX);
    }
    if (mlp) {
      FRESH();
      stage_w1(rw1, lane, j, 84 + 7 * w, 7);
      v2u res[4];
      {
        const unsigned ro = layer == 0 ? (32 * TT + n) * 896 + j * 64 + 8 * h : W_X2 + TT * XB + 2 * j * 1024 + n * 16 + 8 * h;
#pragma unroll
        for (int q = 0; q < 4; ++q)
          res[q] = layer == 0 ? __builtin_amdgcn_raw_buffer_load_b64(rx, ro + 16 * q, 0, 0) : __builtin_amdgcn_raw_buffer_load_b64(rws, ro + (q >> 1) * 1024 + (q & 1) * 512, 0, 16);
      }
      if (w == 0) poll((layer == 0 && lane >= 32) ? flag_line(ws, 0, E_W2, wg) + (lane - 32) : flag_line(ws, layer, E_O, wg) + (lane & 15), epoch, dead);
      __syncthreads();
      STAMP(6);
      FRESH();
      {
        f32x16 a[1];
        unsigned bo0[1] = {tp0 + L_WO}, bo1[1] = {tp1 + L_WO};
        kloop<1, true, 1024>(L, xf, rws, W_O + layer * X_L + TT * XB + lane * 16, bo0, bo1, a, NoBg{});
        STAMP(7);
        MFMA_DONE(a[0]);
        unsigned o[8];
#pragma unroll
        for (int q = 0; q < 4; ++q) {
          o[2 * q] = pk(f32x2{a[0][4 * q], a[0][4 * q + 1]} + unpk(res[q].x));
          o[2 * q + 1] = pk(f32x2{a[0][4 * q + 2], a[0][4 * q + 3]} + unpk(res[q].y));
        }
        const int xo = W_X1 + layer * X_L + TT * XB + 2 * j * 1024 + lane * 16;
        __builtin_amdgcn_raw_buffer_store_b128(v4u{o[0], o[1], o[2], o[3]}, rws, xo, 0, 16);
        __builtin_amdgcn_raw_buffer_store_b128(v4u{o[4], o[5], o[6], o[7]}, rws, xo + 1024, 0, 16);
        float s = 0.f, q2 = 0.f;
#pragma unroll
        for (int e = 0; e < 8; ++e) { s = dot2(o[e], ONES, s); q2 = dot2(o[e], o[e], q2); }
        s += xh(s); q2 += xh(q2);
        __builtin_amdgcn_raw_buffer_store_b64(v2u{__builtin_bit_cast(unsigned, s), __builtin_bit_cast(unsigned, q2)}, rws,
                                              h ? W_END : W_X1 + layer * X_L + TT * XB + X_ST + j * 256 + n * 8, 0, 16);
      }
      VMWAIT(0);
      __syncthreads();
      STAMP(8);
      if (w == 0 && lane < 14) __hip_atomic_store(flag_line(ws, layer, E_X1, c * 16 + lane) + j, epoch, RLX);
      if (layer == 0) w2range<0, 14>(w2f, rws, W2V);
      colsum<4096>(L, tr_w1(lane, 0, w), tr_w1(lane, 1, w), L_CS1 + w * 128, lane);
      if (w == 0) poll(flag_line(ws, layer, E_X1, wg) + (lane < 14 ? lane : 0), epoch, dead);
      __syncthreads();
      STAMP(9); STAMPC(24);
      FRESH();
      {
        v2u st[7];
#pragma unroll
        for (int i = 0; i < 7; ++i) st[i] = __builtin_amdgcn_raw_buffer_load_b64(rws, W_X1 + layer * X_L + TT * XB + X_ST + (7 * h + i) * 256 + n * 8, 0, 16);
        SB();
        f32x16 a[4];
        unsigned b40[4], b41[4];
#pragma unroll
        for (int q = 0; q < 4; ++q) { b40[q] = tr_w1(lane, 0, q); b41[q] = b40[q] + 2048; }
        kloop<4, true, 4096>(L, xf, rws, W_X1 + layer * X_L + TT * XB + lane * 16, b40, b41, a, NoBg{});
        STAMP(10); STAMPC(25);
        mean = 0.f; var = 0.f;
#pragma unroll
        for (int i = 0; i < 7; ++i) { const f32x2 f = __builtin_bit_cast(f32x2, st[i]); mean += f[0]; var += f[1]; }
        mean += xh(mean); var += xh(var);
        mean *= (1.0f / DM);
        var = fmaxf(var * (1.0f / DM) - mean * mean, 0.f);
        rs = __builtin_amdgcn_rsqf(var + 1e-5f);
        rs = rs * __builtin_amdgcn_rsqf(var * rs * rs + 1e-5f);
        const float nmr = -mean * rs;
        asm volatile("s_nop 11" : "+v"(a[0]), "+v"(a[1]), "+v"(a[2]), "+v"(a[3]));
        f32x4 cs[4], bb1[4];
#pragma unroll
        for (int q = 0; q < 4; ++q) { cs[q] = *(LAS f32x4*)(L + L_CS1 + q * 32 + h * 16); bb1[q] = *(LAS f32x4*)(L + M_B1 + q * 32 + h * 16); }
#pragma unroll
        for (int cc = 0; cc < 4; ++cc) {
          unsigned o[8];
          f32x2 v[8], z[8];
#pragma unroll
          for (int e = 0; e < 8; ++e) z[e] = f32x2{cs[e >> 1][2 * (e & 1)], cs[e >> 1][2 * (e & 1) + 1]} * nmr + f32x2{bb1[e >> 1][2 * (e & 1)], bb1[e >> 1][2 * (e & 1) + 1]};
          SB();
          if (cc < 3) {
#pragma unroll
            for (int q = 0; q < 4; ++q) { cs[q] = *(LAS f32x4*)(L + L_CS1 + (cc + 1) * 128 + q * 32 + h * 16); bb1[q] = *(LAS f32x4*)(L + M_B1 + (cc + 1) * 128 + q * 32 + h * 16); }
          }
          if (layer == 0) { if (cc == 0) w2range<14, 25>(w2f, rws, W2V); if (cc == 1) w2range<25, 36>(w2f, rws, W2V); if (cc == 2) w2range<36, 46>(w2f, rws, W2V); }
#pragma unroll
          for (int e = 0; e < 8; ++e) v[e] = f32x2{a[cc][2 * e], a[cc][2 * e + 1]} * rs + z[e];
          SB();
#pragma unroll
          for (int e = 0; e < 8; ++e) z[e] = v[e] * v[e];
          SB();
#pragma unroll
          for (int e = 0; e < 8; ++e) z[e] = z[e] * -0.10294324f - 2.3022082f;
          SB();
#pragma unroll
          for (int e = 0; e < 8; ++e) z[e] *= v[e];
          SB();
#pragma unroll
          for (int e = 0; e < 8; ++e) { z[e][0] = __builtin_amdgcn_exp2f(z[e][0]); z[e][1] = __builtin_amdgcn_exp2f(z[e][1]); }
          SB();
#pragma unroll
          for (int e = 0; e < 8; ++e) z[e] += 1.0f;
          SB();
#pragma unroll
          for (int e = 0; e < 8; ++e) { z[e][0] = __builtin_amdgcn_rcpf(z[e][0]); z[e][1] = __builtin_amdgcn_rcpf(z[e][1]); }
          SB();
#pragma unroll
          for (int e = 0; e < 8; ++e) v[e] *= z[e];
          SB();
#pragma unroll
          for (int e = 0; e < 8; ++e) o[e] = pk(v[e]);
          const int ho = W_H + layer * H_L + TT * HB + (8 * j + 2 * cc) * 1024 + lane * 16;
          __builtin_amdgcn_raw_buffer_store_b128(v4u{o[0], o[1], o[2], o[3]}, rws, ho, 0, 16);
          __builtin_amdgcn_raw_buffer_store_b128(v4u{o[4], o[5], o[6], o[7]}, rws, ho + 1024, 0, 16);
          SB();
        }
      }
      STAMP(21); STAMPC(26);
      VMWAIT(0);
      __syncthreads();
      STAMP(11);
      if (w == 0 && lane < 14) __hip_atomic_store(flag_line(ws, layer, E_H1, c * 16 + lane) + j, epoch, RLX);
      FRESH();
      if (layer == 0) w2range<46, 56>(w2f, rws, W2V);
      const int T5 = 4 * c + 2 * hf5, k5 = 4 * jb + w;
      v2u r5[2][2];
#pragma unroll
      for (int m = 0; m < 2; ++m)
#pragma unroll
        for (int e = 0; e < 2; ++e) r5[m][e] = __builtin_amdgcn_raw_buffer_load_b64(rws, W_X1 + layer * X_L + (T5 + m) * XB + k5 * 1024 + e * 512 + n * 16 + 8 * h, 0, 16);
      const v4u bb2 = __builtin_amdgcn_raw_buffer_load_b128(rb2, k5 * 32 + 16 * h, 0, 0);
      if (w == 0) poll(flag_line(ws, layer, E_H1, wg) + (lane < 14 ? lane : 0), epoch, dead);
      __syncthreads();
      STAMP(12); STAMPC(29);
      {
        f32x16 acc[2][2];
        const unsigned hv = W_H + layer * H_L + T5 * HB + 28 * w * 1024 + lane * 16;
        auto ldh = [&](int i) {
          xf[2 * (i % 6)] = __builtin_amdgcn_raw_buffer_load_b128(rws, hv + (i & 3) * 1024, (i >> 2) * 4096, 16);
          xf[2 * (i % 6) + 1] = __builtin_amdgcn_raw_buffer_load_b128(rws, hv + (i & 3) * 1024, (i >> 2) * 4096 + HB, 16);
        };
#pragma unroll
        for (int i = 0; i < 5; ++i) ldh(i);
        SB();
#pragma unroll
        for (int i = 0; i < 28; ++i) {
#pragma unroll
          for (int m = 0; m < 2; ++m) {
#pragma unroll
            for (int x = 0; x < 2; ++x) { if (i == 0) mfma_a0(acc[x][m], w2f[i][x], xf[m]); else mfma_av(acc[x][m], w2f[i][x], xf[2 * (i % 6) + m]); }
            SB();
            if (m == 0 && i + 5 < 28) ldh(i + 5);
            SB();
          }
        }
        STAMP(13); STAMPC(30);
        FRESH();
        if (layer == 0) {
          stage_img(rqkv, lane, 2688, 896 + QCOL, 0, w, c & 1);
          stage_img(rqkv, lane, 2688, 1792 + QCOL, U, w, c & 1);
        }
        asm volatile("s_nop 11" : "+v"(acc[0][0]), "+v"(acc[0][1]), "+v"(acc[1][0]), "+v"(acc[1][1]));
#pragma unroll
        for (int d = 0; d < 4; ++d)
#pragma unroll
          for (int x = 0; x < 2; ++x)
#pragma unroll
            for (int m = 0; m < 2; ++m)
              *(LAS f32x4*)(L + L_RED + (d * 4 + w) * 4096 + (x * 2 + m) * 1024 + lane * 16) = f32x4{acc[x][m][4 * d], acc[x][m][4 * d + 1], acc[x][m][4 * d + 2], acc[x][m][4 * d + 3]};
        __syncthreads();
        f32x4 p[2][2];
#pragma unroll
        for (int s = 0; s < 4; ++s)
#pragma unroll
          for (int x = 0; x < 2; ++x)
#pragma unroll
            for (int m = 0; m < 2; ++m) { const f32x4 t = *(LAS f32x4*)(L + L_RED + (w * 4 + s) * 4096 + (x * 2 + m) * 1024 + lane * 16); p[x][m] = s == 0 ? t : p[x][m] + t; }
#pragma unroll
        for (int m = 0; m < 2; ++m) {
          const unsigned rr[4] = {r5[m][0].x, r5[m][0].y, r5[m][1].x, r5[m][1].y};
          v4u o;
#pragma unroll
          for (int d = 0; d < 4; ++d) o[d] = pk(f32x2{p[d >> 1][m][2 * (d & 1)], p[d >> 1][m][2 * (d & 1) + 1]} + unpk(rr[d]) + unpk(bb2[d]));
          if (layer == 0) {
            __builtin_amdgcn_raw_buffer_store_b128(o, rws, W_X2 + (4 * c + 2 * hf5 + m) * XB + (4 * jb + w) * 1024 + lane * 16, 0, 16);
            float s = 0.f, q2 = 0.f;
#pragma unroll
            for (int e = 0; e < 4; ++e) { s = dot2(o[e], ONES, s); q2 = dot2(o[e], o[e], q2); }
            *(LAS f32x2*)(L + L_RST + ((m * 8 + w * 2 + h) * 32 + n) * 8) = f32x2{s, q2};
          } else {
            __builtin_amdgcn_raw_buffer_store_b128(o, rout, (32 * (4 * c + 2 * hf5 + m) + n) * 896 + (4 * jb + w) * 32 + 16 * h, 0, 0);
          }
        }
      }
    } else if (layer == 1 && wg == 15 && w == 0) {
      bool ok = false;
      for (int spins = 0; spins < SPIN_CAP && !ok; ++spins) {
        bool a = true;
#pragma unroll
        for (int i = 0; i < 4; ++i) a &= __hip_atomic_load((GAS u64*)(ws + W_STARTED) + lane + 64 * i, RLX) == epoch;
        ok = __builtin_amdgcn_ballot_w64(!a) == 0;
      }
      if (lane == 0) __hip_atomic_store((GAS u64*)(ws + W_E), epoch, RLX);
    }
    if (layer == 0) {
      FRESH();
      __syncthreads();
      if (mlp) {
        if (w == 0) {
          f32x2 t = {0.f, 0.f};
#pragma unroll
          for (int i = 0; i < 8; ++i) t += *(LAS f32x2*)(L + L_RST + ((h * 8 + i) * 32 + n) * 8);
          __builtin_amdgcn_raw_buffer_store_b64(__builtin_bit_cast(v2u, t), rws, W_X2 + (4 * c + 2 * hf5 + h) * XB + X_ST + jb * 256 + n * 8, 0, 16);
        }
      } else {
        stage_img(rqkv, lane, 2688, 896 + QCOL, 0, w, c & 1);
        stage_img(rqkv, lane, 2688, 1792 + QCOL, U, w, c & 1);
      }
      VMWAIT(0);
      __syncthreads();
      STAMP(14);
      if (w == 0 && mlp && lane < 16) __hip_atomic_store(flag_line(ws, 0, E_X2, c * 16 + lane) + j, epoch, RLX);
      stage_img(rqkv, lane, 2688, QCOL, 2 * U, w, c & 1);
      if (w == 0) poll(flag_line(ws, 0, E_X2, wg) + (lane < 14 ? lane : 0), epoch, dead);
      __syncthreads();
      STAMP(15);
    }
    STAMP(16);
  }
}
}

extern "C" void kernel_launch(void* const* d_in, const int* in_sizes, int n_in, void* d_out, int out_size, void* d_ws, size_t ws_size, hipStream_t stream) {
  (void)hipFuncSetAttribute((const void*)mk::fused, hipFuncAttributeMaxDynamicSharedMemorySize, mk::L_TOTAL);
  mk::fused<<<256, 256, mk::L_TOTAL, stream>>>((const unsigned short*)d_in[0], (const unsigned short*)d_in[1], (const unsigned short*)d_in[2],
      (const unsigned short*)d_in[4], (const unsigned short*)d_in[5], (const unsigned short*)d_in[6], (const unsigned short*)d_in[7],
      (unsigned short*)d_out, (unsigned char*)d_ws);
}
```

```cpp
#include <hip/hip_runtime.h>
#include <stdint.h>

namespace mk {
typedef unsigned v4u __attribute__((ext_vector_type(4)));
typedef unsigned v2u __attribute__((ext_vector_type(2)));
typedef float f32x4 __attribute__((ext_vector_type(4)));
typedef float f32x2 __attribute__((ext_vector_type(2)));
typedef float f32x16 __attribute__((ext_vector_type(16)));
typedef short s16x4 __attribute__((ext_vector_type(4)));
typedef __bf16 bf16x8 __attribute__((ext_vector_type(8)));
typedef __bf16 bf16x2 __attribute__((ext_vector_type(2)));
typedef unsigned long long u64;
typedef __amdgpu_buffer_rsrc_t rsrc_t;
#define LAS __attribute__((address_space(3)))
#define GAS __attribute__((address_space(1)))
#define RLX __ATOMIC_RELAXED, __HIP_MEMORY_SCOPE_AGENT
#define DI __device__ __forceinline__
#define SB() __builtin_amdgcn_sched_barrier(0)
#define OPAQUE(x) asm volatile("" : "+v"(x))

constexpr int SEQ = 2048, DM = 448, FF = 1792;
constexpr int U = 28672;
constexpr int L_K = 3 * U, L_V = L_K + 22 * 1024, L_WO = L_K + 46 * 1024, L_MISC = L_WO + U, L_TOTAL = 163840;
constexpr int L_RED = 2 * U, L_RST = L_RED + 65536;
constexpr int M_CSQ = L_MISC, M_CS1 = L_MISC + 384, M_B1 = L_MISC + 896, M_ST = L_MISC + 1408;
static_assert(L_MISC + 2048 == L_TOTAL, "LDS map");
constexpr int W_E = 0, W_STARTED = 4096, W_FLAG = 8192;
enum { E_KV = 0  , E_O = 4, E_X1 = 5, E_H0 = 6, E_H1 = 7, E_X = 8, E_W2 = 9  , E_W1 = 11, E_WQ = 12, NEDGE = 13 };
constexpr int W_KV = 1 << 20, KV_L = 16 * 64 * 4096;
constexpr int XB = 32 * 1024, X_L = 64 * XB;
constexpr int X_ST = 28 * 1024;
constexpr int W_O = W_KV + 2 * KV_L, W_X1 = W_O + 2 * X_L, W_X = W_X1 + 2 * X_L, W_H = W_X + 2 * X_L;
constexpr int HB = 112 * 1024, H_L = 64 * HB;
constexpr int W_W2T = W_H + 2 * H_L;
constexpr int W_W1T = W_W2T + 7 * 112 * 2048;
constexpr int W_WQT = W_W1T + 14 * 112 * 1024;
constexpr int W_WOT = W_WQT + 16 * 3 * U;
constexpr int W_END = W_WOT + 14 * U;
constexpr int W_STAMP = W_END;
static_assert(W_FLAG + 2 * 256 * NEDGE * 128 <= W_KV, "flags");
constexpr int SPIN_CAP = 1 << 16;
constexpr unsigned ONES = 0x3f803f80u;

DI f32x16 mfma(v4u a, v4u b, f32x16 c) {
  return __builtin_amdgcn_mfma_f32_32x32x16_bf16(__builtin_bit_cast(bf16x8, a), __builtin_bit_cast(bf16x8, b), c, 0, 0, 0);
}
DI void mfma_v0(f32x16& acc, v4u a, v4u b) { asm volatile("v_mfma_f32_32x32x16_bf16 %0, %1, %2, 0" : "=&v"(acc) : "v"(a), "v"(b)); }
DI void mfma_vv(f32x16& acc, v4u a, v4u b) { asm volatile("v_mfma_f32_32x32x16_bf16 %0, %1, %2, %0" : "+v"(acc) : "v"(a), "v"(b)); }
DI void mfma_a0(f32x16& acc, v4u a, v4u b) { asm volatile("v_mfma_f32_32x32x16_bf16 %0, %1, %2, 0" : "=&v"(acc) : "a"(a), "v"(b)); }
DI void mfma_av(f32x16& acc, v4u a, v4u b) { asm volatile("v_mfma_f32_32x32x16_bf16 %0, %1, %2, %0" : "+v"(acc) : "a"(a), "v"(b)); }
#define MFMA_DONE(x) asm volatile("s_nop 11" : "+v"(x))
DI v2u trd(LAS unsigned char* L, unsigned a) {
  return __builtin_bit_cast(v2u, __builtin_amdgcn_ds_read_tr16_b64_v4i16((LAS s16x4*)(L + a)));
}
DI v4u trf(LAS unsigned char* L, unsigned a0, unsigned a1) { const v2u p = trd(L, a0), q = trd(L, a1); return v4u{p.x, p.y, q.x, q.y}; }
DI unsigned pk(float a, float b) { f32x2 f = {a, b}; return __builtin_bit_cast(unsigned, __builtin_convertvector(f, bf16x2)); }
DI unsigned pk(f32x2 f) { return __builtin_bit_cast(unsigned, __builtin_convertvector(f, bf16x2)); }
DI float blo(unsigned u) { return __builtin_bit_cast(float, u << 16); }
DI float bhi(unsigned u) { return __builtin_bit_cast(float, u & 0xffff0000u); }
DI f32x2 unpk(unsigned u) { return f32x2{blo(u), bhi(u)}; }
DI int rfl(int x) { return __builtin_amdgcn_readfirstlane(x); }
DI float dot2(unsigned a, unsigned b, float c) { return __builtin_amdgcn_fdot2_f32_bf16(__builtin_bit_cast(bf16x2, a), __builtin_bit_cast(bf16x2, b), c, false); }
DI float max3(float a, float b, float c) { float r; asm("v_max3_f32 %0, %1, %2, %3" : "=v"(r) : "v"(a), "v"(b), "v"(c)); return r; }
DI float xh(float v) {
  const unsigned u = __builtin_bit_cast(unsigned, v);
  const auto r = __builtin_amdgcn_permlane32_swap(u, u, false, false);
  return __builtin_bit_cast(float, (threadIdx.x & 32) ? r[0] : r[1]);
}
DI rsrc_t mkrs(const void* p, int bytes) { return __builtin_amdgcn_make_buffer_rsrc((void*)p, 0, bytes, 0x00020000); }
DI void dma16(rsrc_t rs, unsigned voff, unsigned soff, unsigned ldst) {
  asm volatile("s_mov_b32 m0, %0\n\ts_nop 0\n\tbuffer_load_dwordx4 %1, %2, %3 offen lds" :: "s"(ldst), "v"(voff), "s"(rs), "s"(soff) : "memory");
}
DI void dma16c(rsrc_t rs, unsigned voff, unsigned soff, unsigned ldst) {
  asm volatile("s_mov_b32 m0, %0\n\ts_nop 0\n\tbuffer_load_dwordx4 %1, %2, %3 offen sc1 lds" :: "s"(ldst), "v"(voff), "s"(rs), "s"(soff) : "memory");
}
template <int N> DI void dmaN(rsrc_t rs, unsigned voff, unsigned soff, unsigned ldst) {
  if constexpr (N == 4)
    asm volatile("s_mov_b32 m0, %0\n\ts_nop 0\n\tbuffer_load_dwordx4 %1, %2, %3 offen sc1 lds\n\tbuffer_load_dwordx4 %1, %2, %3 offen offset:1024 sc1 lds\n\t"
                 "buffer_load_dwordx4 %1, %2, %3 offen offset:2048 sc1 lds\n\tbuffer_load_dwordx4 %1, %2, %3 offen offset:3072 sc1 lds" :: "s"(ldst), "v"(voff), "s"(rs), "s"(soff) : "memory");
  else if constexpr (N == 3)
    asm volatile("s_mov_b32 m0, %0\n\ts_nop 0\n\tbuffer_load_dwordx4 %1, %2, %3 offen sc1 lds\n\tbuffer_load_dwordx4 %1, %2, %3 offen offset:1024 sc1 lds\n\t"
                 "buffer_load_dwordx4 %1, %2, %3 offen offset:2048 sc1 lds" :: "s"(ldst), "v"(voff), "s"(rs), "s"(soff) : "memory");
  else
    asm volatile("s_mov_b32 m0, %0\n\ts_nop 0\n\tbuffer_load_dwordx4 %1, %2, %3 offen sc1 lds\n\tbuffer_load_dwordx4 %1, %2, %3 offen offset:1024 sc1 lds"
                 :: "s"(ldst), "v"(voff), "s"(rs), "s"(soff) : "memory");
}
DI void copy_in(rsrc_t rs, int lane, unsigned src, unsigned dst, int cnt) {
#pragma unroll 1
  for (int i = 0; i < cnt; ++i) dma16c(rs, lane * 16, src + i * 1024, dst + i * 1024);
}
#define METER 5
#define STR2(x) #x
#define STR(x) STR2(x)
#define VMWAIT(n) asm volatile("s_waitcnt vmcnt(" #n ")" ::: "memory")
#define ST16(v, off) __builtin_amdgcn_raw_buffer_store_b128(v, rws, off, 0, 16)
#define ST8(v, off) __builtin_amdgcn_raw_buffer_store_b64(v, rws, off, 0, 16)
#define LD8(off) __builtin_amdgcn_raw_buffer_load_b64(rws, off, 0, 16)
DI unsigned tr_base(int lane, int tp, bool nat, int par) {
  const int h = lane >> 5, cb = (lane >> 4) & 1, q = (lane >> 2) & 3, p = lane & 3;
  const int row = nat ? 8 * h + 4 * tp + q : 8 * tp + 4 * h + q;
  return row * 64 + ((4 * cb + p + par) & 7) * 8;
}
DI void xwait6(v4u& x) { asm volatile("s_waitcnt vmcnt(6)" : "+v"(x) :: "memory"); }
template <int I> DI void xload(v4u& x, rsrc_t rs, unsigned voff, unsigned s0, unsigned s1) {
  asm volatile("buffer_load_dwordx4 %0, %1, %2, %3 offen offset:%4 sc1" : "=v"(x) : "v"(voff), "s"(rs), "s"(I < 4 ? s0 : s1), "i"((I & 3) * 1024) : "memory");
}
template <int I> DI void w2load(v4u (&w2f)[28][2], rsrc_t rs, unsigned voff) {
  asm volatile("buffer_load_dwordx4 %0, %1, %2, %3 offen offset:%4 sc1" : "=a"(w2f[I >> 1][I & 1]) : "v"(voff), "s"(rs),
               "s"(((I >> 1) < 14 ? 8 * (I >> 1) : 8 * ((I >> 1) - 14) + 4) * 2048), "i"((I & 1) * 1024) : "memory");
}
template <int LO, int HI> DI void w2range(v4u (&w2f)[28][2], rsrc_t rs, unsigned voff) {
  if constexpr (LO < HI) { w2load<LO>(w2f, rs, voff); w2range<LO + 1, HI>(w2f, rs, voff); }
}
template <int TR1> DI v4u wfrag(LAS unsigned char* L, unsigned b01, unsigned b2, int s, int q) {
  if constexpr (TR1 == 0) return *(LAS v4u*)(L + b01 + (s * 4 + q) * 1024);
  else { const unsigned a = q < 2 ? b01 + q * U + s * 1024 : b2 + s * 1024; return trf(L, a, a + TR1); }
}
template <int NC, int TR1, int S> DI void kstep(LAS unsigned char* L, v4u (&xr)[7], rsrc_t rs, unsigned voff, unsigned s0, unsigned s1, unsigned b01, unsigned b2,
                                                v4u (&wb)[NC == 1 ? 7 : NC], f32x16 (&acc)[NC]) {
  if constexpr (S < 7) {
    xwait6(xr[S]);
    SB();
#pragma unroll
    for (int q = 0; q < NC; ++q) {
      mfma_vv(acc[q], wb[NC == 1 ? S : q], xr[S]);
      SB();
      wb[NC == 1 ? S : q] = wfrag<TR1>(L, b01, b2, S + (NC == 1 ? 7 : 1), q);
      if (q == NC - 1) xload<S>(xr[S], rs, voff, s0, s1);
      SB();
    }
    kstep<NC, TR1, S + 1>(L, xr, rs, voff, s0, s1, b01, b2, wb, acc);
  }
}
template <int I> DI void xfirst(v4u (&xr)[7], rsrc_t rs, unsigned voff) { if constexpr (I < 7) { xload<I>(xr[I], rs, voff, 0u, 4096u); xfirst<I + 1>(xr, rs, voff); } }
template <int NC, int TR1, class F> DI void kloop(LAS unsigned char* L, rsrc_t rs, unsigned voff, unsigned b01, unsigned b2, f32x16 (&acc)[NC], F hook) {
  v4u xr[7], wb[NC == 1 ? 7 : NC];
  xfirst<0>(xr, rs, voff);
  hook();
  OPAQUE(b01); OPAQUE(b2);
#pragma unroll
  for (int i = 0; i < (NC == 1 ? 7 : NC); ++i) wb[i] = wfrag<TR1>(L, b01, b2, NC == 1 ? i : 0, NC == 1 ? 0 : i);
#pragma unroll
  for (int q = 0; q < NC; ++q) {
#pragma unroll
    for (int r = 0; r < 16; ++r) acc[q][r] = 0.f;
    asm volatile("" : "+v"(acc[q]));
  }
#pragma unroll 1
  for (int it = 1; it <= 4; ++it) {
    const unsigned s0 = it < 4 ? it * 7168u : (unsigned)W_END;
    kstep<NC, TR1, 0>(L, xr, rs, voff, s0, s0 + 4096u, b01, b2, wb, acc);
    b01 += TR1 == 0 ? 7 * 4096 : 7 * 1024; b2 += 7 * 1024;
  }
  asm volatile("s_waitcnt vmcnt(0)" : "+v"(xr[0]), "+v"(xr[1]), "+v"(xr[2]), "+v"(xr[3]), "+v"(xr[4]), "+v"(xr[5]), "+v"(xr[6]) :: "memory");
}
template <int TR1> DI void colsum(LAS unsigned char* L, unsigned b, int rounds, f32x16& a) {
  const v4u ones = {ONES, ONES, ONES, ONES};
#pragma unroll 1
  for (int s = 0; s < rounds; ++s) {
    OPAQUE(b);
    v4u f[7];
#pragma unroll
    for (int i = 0; i < 7; ++i) f[i] = wfrag<TR1>(L, b, b, i, 0);
    SB();
#pragma unroll
    for (int i = 0; i < 7; ++i) a = mfma(f[i], ones, a);
    b += TR1 == 0 ? 7 * 4096 : 7 * 1024;
  }
}
DI void colsum_put(LAS unsigned char* L, const f32x16& a, unsigned dst, int lane) {
  if ((lane & 31) == 0) {
#pragma unroll
    for (int q = 0; q < 4; ++q) *(LAS f32x4*)(L + dst + q * 32 + (lane >> 5) * 16) = f32x4{a[4 * q], a[4 * q + 1], a[4 * q + 2], a[4 * q + 3]};
  }
}
DI f32x16 zero16() { f32x16 a;
#pragma unroll
  for (int r = 0; r < 16; ++r) a[r] = 0.f;
  return a; }
DI GAS u64* flag_line(unsigned char* ws, int layer, int edge, int wg) { return (GAS u64*)(ws + W_FLAG + ((layer * 256 + wg) * NEDGE + edge) * 128); }
DI void poll(GAS u64* p, u64 epoch, bool& dead) {
  if (dead) return;
  for (int spins = 0;; ++spins) {
    const u64 f = __hip_atomic_load(p, RLX);
    if (__builtin_amdgcn_ballot_w64(f != epoch) == 0) break;
    if (spins > SPIN_CAP) { dead = true; break; }
  }
  __builtin_amdgcn_fence(__ATOMIC_ACQUIRE, "wavefront");
}
#define CHECK(f, p) do { if (__builtin_amdgcn_ballot_w64((f) != epoch) != 0) poll(p, epoch, dead); __builtin_amdgcn_fence(__ATOMIC_ACQUIRE, "wavefront"); } while (0)
#define STAMP(i)
#define STAMPC(i)
DI void put_k(LAS unsigned char* L, int lt, int lane, v4u a, v4u b) {
  *(LAS v4u*)(L + L_K + lt * 2048 + lane * 16) = a;
  *(LAS v4u*)(L + L_K + lt * 2048 + 1024 + lane * 16) = b;
}
DI void put_v(LAS unsigned char* L, int lt, int lane, v4u a, v4u b) {
  const unsigned o = L_V + lt * 2048 + (lane & 31) * 64 + (lane >> 5) * 8;
  *(LAS v2u*)(L + o) = v2u{a.x, a.y};
  *(LAS v2u*)(L + o + 16) = v2u{a.z, a.w};
  *(LAS v2u*)(L + o + 32) = v2u{b.x, b.y};
  *(LAS v2u*)(L + o + 48) = v2u{b.z, b.w};
}
DI void ln_pack(LAS unsigned char* L, const f32x16& a, unsigned cs, int h, float nmsc, float sc, float pad0, v4u& o0, v4u& o1) {
  unsigned o[8];
  f32x4 cq[4];
  f32x2 t[8];
#pragma unroll
  for (int q = 0; q < 4; ++q) cq[q] = *(LAS f32x4*)(L + cs + q * 32 + h * 16);
#pragma unroll
  for (int e = 0; e < 8; ++e) t[e] = f32x2{a[2 * e], a[2 * e + 1]} * sc;
  SB();
#pragma unroll
  for (int e = 0; e < 8; ++e) t[e] += f32x2{cq[e >> 1][2 * (e & 1)], cq[e >> 1][2 * (e & 1) + 1]} * nmsc;
  SB();
#pragma unroll
  for (int e = 0; e < 8; ++e) o[e] = pk(t[e]);
  if (h == 1) { o[6] = pk(pad0, 0.f); o[7] = 0u; }
  o0 = v4u{o[0], o[1], o[2], o[3]}; o1 = v4u{o[4], o[5], o[6], o[7]};
}
DI void row_stats(const v2u (&st)[7], float& mean, float& var) {
  f32x2 t = __builtin_bit_cast(f32x2, st[0]);
#pragma unroll
  for (int i = 1; i < 7; ++i) t += __builtin_bit_cast(f32x2, st[i]);
  mean = (t[0] + xh(t[0])) * (1.0f / DM);
  var = fmaxf((t[1] + xh(t[1])) * (1.0f / DM) - mean * mean, 0.f);
}

__global__ void __launch_bounds__(256, 1) fused(const unsigned short* __restrict__ xin, const unsigned short* __restrict__ wqkv, const unsigned short* __restrict__ wo,
    const unsigned short* __restrict__ w1, const unsigned short* __restrict__ b1, const unsigned short* __restrict__ w2, const unsigned short* __restrict__ b2,
    unsigned short* __restrict__ out, unsigned char* ws) {
  extern __shared__ __attribute__((aligned(16))) unsigned char smem[];
  LAS unsigned char* L = (LAS unsigned char*)smem;
  int lane = threadIdx.x & 63, w = rfl(threadIdx.x >> 6), n = lane & 31, h = lane >> 5;
  int c = 2 * (blockIdx.x & 7) + (blockIdx.x >> 7), j = (blockIdx.x >> 3) & 15;
  const bool mlp = j < 14;
  const int c0_ = c, j0_ = j, w0_ = w, lane0_ = lane;
#define wg (c * 16 + j)
#define TT (4 * c + w)
#define hf5 (j >= 7 ? 1 : 0)
#define jb (j >= 7 ? j - 7 : j)
#define W2V (W_W2T + (jb * 112 + w) * 2048 + lane * 16)
#define W2PART(i) do { if ((i) == 0) w2range<0, 3>(w2f, rws, W2V); if ((i) == 1) w2range<3, 6>(w2f, rws, W2V); if ((i) == 2) w2range<6, 9>(w2f, rws, W2V); \
    if ((i) == 3) w2range<9, 12>(w2f, rws, W2V); if ((i) == 4) w2range<12, 15>(w2f, rws, W2V); if ((i) == 5) w2range<15, 18>(w2f, rws, W2V); \
    if ((i) == 6) w2range<18, 21>(w2f, rws, W2V); if ((i) == 7) w2range<21, 24>(w2f, rws, W2V); } while (0)
#define FRESH() do { c = c0_; j = j0_; w = w0_; lane = lane0_; asm volatile("" : "+s"(c), "+s"(j), "+s"(w)); asm volatile("" : "+v"(lane)); n = lane & 31; h = lane >> 5; } while (0)
  v4u xa[4][2];
  {
    const rsrc_t r0 = w == 0 ? mkrs(xin, mlp ? SEQ * DM * 2 : 0) : mkrs(wqkv, DM * 3 * DM * 2);
    const unsigned vo = w == 0 ? (128 * c + n) * 896 + 64 * j + 16 * h : (16 * c + (lane >> 2)) * 2688 + (lane & 3) * 16 + (w == 3 ? 0 : 896 * w) + ((56 * j) & ~15);
    xa[0][0] = __builtin_amdgcn_raw_buffer_load_b128(r0, vo, 0, 0);
    xa[0][1] = __builtin_amdgcn_raw_buffer_load_b128(r0, vo, w == 0 ? 32 : 256 * 2688, 0);
    if (w == 0) {
#pragma unroll
      for (int t = 1; t < 4; ++t)
#pragma unroll
        for (int e = 0; e < 2; ++e) xa[t][e] = __builtin_amdgcn_raw_buffer_load_b128(r0, vo + 32 * e, t * 32 * 896, 0);
    } else if (mlp) {
      if (w == 3) {
        const rsrc_t ro = mkrs(wo, DM * DM * 2);
        xa[1][0] = __builtin_amdgcn_raw_buffer_load_b128(ro, (16 * c + (lane >> 2)) * 896 + (lane & 3) * 16 + j * 64, 0, 0);
        xa[1][1] = __builtin_amdgcn_raw_buffer_load_b128(ro, (16 * c + (lane >> 2)) * 896 + (lane & 3) * 16 + j * 64, 256 * 896, 0);
        xa[2][0].x = *(const unsigned*)(b1 + 128 * j + 2 * lane);
      } else if (w == 1) {
        const rsrc_t rw2 = mkrs(w2, FF * DM * 2);
#pragma unroll 1
        for (int u = 0; u < 8; ++u) dma16(rw2, (lane >> 3) * 896 + (lane & 7) * 16, ((7 * c + 4 * hf5) * 16 + 8 * u) * 896 + jb * 128, L_K + u * 1024);
      } else {
        const rsrc_t rw1 = mkrs(w1, DM * FF * 2);
#pragma unroll 1
        for (int u = 0; u < 7; ++u) { const int f = 7 * c + u; dma16(rw1, (lane >> 2) * 3584 + (lane & 3) * 16, (f >> 2) * 16 * 3584 + j * 256 + (f & 3) * 64, L_K + 8192 + u * 1024); }
      }
    }
  }
  SB();
  const rsrc_t rws = mkrs(ws, W_END);
  int layer = 0;
  STAMP(0);
  const u64 epoch = *(const u64*)(ws + W_E) + 1;
  bool dead = false;
  v4u w2f[28][2];
#pragma unroll
  for (int i = 0; i < 28; ++i) { asm volatile("" : "=a"(w2f[i][0])); asm volatile("" : "=a"(w2f[i][1])); }
  if (w == 0) {
    if (mlp) {
#pragma unroll
      for (int t = 0; t < 4; ++t) {
        const int xo = W_X + (4 * c + t) * XB;
        ST16(xa[t][0], xo + 2 * j * 1024 + lane * 16);
        ST16(xa[t][1], xo + 2 * j * 1024 + 1024 + lane * 16);
        float s = 0.f, q2 = 0.f;
#pragma unroll
        for (int e = 0; e < 8; ++e) { s = dot2(xa[t][e >> 2][e & 3], ONES, s); q2 = dot2(xa[t][e >> 2][e & 3], xa[t][e >> 2][e & 3], q2); }
        s += xh(s); q2 += xh(q2);
        ST8((v2u{__builtin_bit_cast(unsigned, s), __builtin_bit_cast(unsigned, q2)}), h ? W_END : xo + X_ST + j * 256 + n * 8);
      }
    }
  } else {
    const int o = W_WQT + (j * 3 + w - 1) * U + c * 1024 + lane * 16;
    ST16(xa[0][0], o);
    ST16(xa[0][1], c < 12 ? o + 16384 : W_END);
    if (mlp && w == 3) {
      const int o2 = W_WOT + j * U + c * 1024 + lane * 16;
      ST16(xa[1][0], o2);
      ST16(xa[1][1], c < 12 ? o2 + 16384 : W_END);
      *(LAS f32x2*)(L + M_B1 + lane * 8) = unpk(xa[2][0].x);
    }
  }
  STAMP(22);
  VMWAIT(0);
  __builtin_amdgcn_s_barrier();
  if (w == 0) {
    if (lane < 16 ? mlp : lane < 32) __hip_atomic_store(lane < 16 ? flag_line(ws, 0, E_X, c * 16 + lane) + j : flag_line(ws, 0, E_WQ, (lane & 15) * 16 + j) + c, epoch, RLX);
    if (lane == 0) __hip_atomic_store((GAS u64*)(ws + W_STARTED) + wg, epoch, RLX);
    STAMP(17);
    poll(lane < 16 ? flag_line(ws, 0, E_X, wg) + (lane < 14 ? lane : 0) : flag_line(ws, 0, E_WQ, wg) + (lane & 15), epoch, dead);
    STAMP(18);
  } else if (mlp) {
    if (w == 1) {
      const unsigned ta = L_K + (4 * h + ((lane >> 2) & 3)) * 128 + (8 * ((lane >> 4) & 1) + 4 * ((lane >> 1) & 1) + 2 * (lane & 1)) * 8;
#pragma unroll 1
      for (int u = 0; u < 8 - 2 * hf5; ++u)
        ST16(trf(L, ta + (u >> 1) * 2048 + (u & 1) * 8, ta + (u >> 1) * 2048 + (u & 1) * 8 + 1024), W_W2T + (jb * 112 + 7 * c + 4 * hf5) * 2048 + u * 1024 + lane * 16);
    } else if (w == 2) {
      const unsigned tb = L_K + 8192 + tr_base(lane, 0, false, 0);
#pragma unroll 1
      for (int u = 0; u < 7; ++u) ST16(trf(L, tb + u * 1024, tb + u * 1024 + 512), W_W1T + (j * 112 + 7 * c + u) * 1024 + lane * 16);
    }
  }
  __builtin_amdgcn_s_barrier();
#pragma unroll 1
  for (int i = 0; i < 7; ++i) dmaN<3>(rws, lane * 16, W_WQT + j * 3 * U + (21 * w + 3 * i) * 1024, (21 * w + 3 * i) * 1024);
  VMWAIT(0);
  __syncthreads();
  if (w == 0 && mlp && lane < 48)
    __hip_atomic_store(lane < 32 ? flag_line(ws, 0, E_W2, (lane >> 1) * 16 + jb + 7 * (lane & 1)) + 2 * c + hf5 : flag_line(ws, 0, E_W1, (lane & 15) * 16 + j) + c, epoch, RLX);

  for (; layer < 2; ++layer) {
    FRESH();
    float mean, var;
    v4u qf[2], kf[2];
    f32x16 sc[9];
    int oaddr[4];
    u64 fw;
    GAS u64* fwp = flag_line(ws, 0, E_W2, wg) + ((mlp && lane < 48) ? lane : 48);
    {
      STAMP(1);
      v2u st[7];
#pragma unroll
      for (int i = 0; i < 7; ++i) st[i] = LD8(W_X + layer * X_L + TT * XB + X_ST + (7 * h + i) * 256 + n * 8);
      SB();
      f32x16 a[3];
      const unsigned tq = tr_base(lane, 0, true, j & 1);
      kloop<3, 256>(L, rws, W_X + layer * X_L + TT * XB + lane * 16, tq, tq + 2 * U, a, [&]() {
        if (layer == 0 && w < 3) { f32x16 ca = zero16(); colsum<256>(L, tq + w * U, 4, ca); colsum_put(L, ca, M_CSQ + w * 128, lane); }
      });
      if (layer == 0) __syncthreads();
      STAMP(23);
      if (layer == 0) fw = __hip_atomic_load(fwp, RLX);
      row_stats(st, mean, var);
      const float rs = __builtin_amdgcn_rsqf(var + 1e-5f);
      asm volatile("s_nop 7" : "+v"(a[0]), "+v"(a[1]), "+v"(a[2]));
      const int kvoff = W_KV + layer * KV_L + (j * 64 + TT) * 4096 + lane * 16;
      v4u v0, v1;
      ln_pack(L, a[0], M_CSQ, h, -mean * rs, rs, 0.f, kf[0], kf[1]);
      ST16(kf[0], kvoff); ST16(kf[1], kvoff + 1024);
      ln_pack(L, a[1], M_CSQ + 128, h, -mean * rs, rs, 1.f, v0, v1);
      ST16(v0, kvoff + 2048); ST16(v1, kvoff + 3072);
      STAMP(19);
      const float sq = rs * (0.18898223650461362f * 1.4426950408889634f);
      ln_pack(L, a[2], M_CSQ + 256, h, -mean * sq, sq, 0.f, qf[0], qf[1]);
      if (w < 3) put_k(L, 8 + w, lane, kf[0], kf[1]);
      put_v(L, 8 + w, lane, v0, v1);
#pragma unroll
      for (int r = 0; r < 16; ++r) {
        const bool past = (r & 3) + 8 * (r >> 2) + 4 * h > n;
        sc[0][r] = past ? 0.f : -1e30f;
        sc[8][r] = past ? -1e30f : 0.f;
      }
      asm volatile("s_nop 1" : "+v"(sc[0]), "+v"(sc[8]), "+v"(qf[0]), "+v"(qf[1]));
      mfma_vv(sc[8], kf[0], qf[0]); mfma_vv(sc[8], kf[1], qf[1]);
      VMWAIT(0);
      if (lane < 2 && c + 1 + lane < 16) __hip_atomic_store(flag_line(ws, layer, E_KV + (lane ? 0 : 2) + (w >> 1), wg + 16 * (1 + lane)) + (w & 1), epoch, RLX);
      STAMP(2);
      if (mlp && layer == 0) { dmaN<4>(rws, lane * 16, W_WOT + j * U + 7 * w * 1024, L_WO + 7 * w * 1024); dmaN<3>(rws, lane * 16, W_WOT + j * U + (7 * w + 4) * 1024, L_WO + (7 * w + 4) * 1024); }
#pragma unroll
      for (int q = 0; q < 4; ++q) {
        const int P = 7 * j + 2 * q + h;
        oaddr[q] = (q == 3 && h == 1) ? W_END : W_O + layer * X_L + TT * XB + n * 16 + (P >> 2) * 1024 + (P & 1) * 512 + ((P >> 1) & 1) * 8;
        OPAQUE(oaddr[q]);
      }
      if (4 * c - 8 + 2 * w >= 0) poll(flag_line(ws, layer, E_KV + w, wg) + (lane & 1), epoch, dead);
      STAMP(3);
    }
    FRESH();
    unsigned tp0 = tr_base(lane, 0, false, 0);
    {
      v4u hk[2][4];
      const int ho = 4 * c - 8 + 2 * w >= 0 ? W_KV + layer * KV_L + (j * 64 + 4 * c - 8 + 2 * w) * 4096 : W_END;
#pragma unroll
      for (int hh = 0; hh < 2; ++hh)
#pragma unroll
        for (int e = 0; e < 4; ++e) hk[hh][e] = __builtin_amdgcn_raw_buffer_load_b128(rws, lane * 16 + e * 1024, ho + hh * 4096, 16);
      float mx[4] = {-1e30f, -1e30f, -1e30f, -1e30f};
#define TMAX(T, a, b) do { _Pragma("unroll") for (int r_ = a; r_ < b; r_ += 2) mx[(r_ >> 1) & 3] = max3(mx[(r_ >> 1) & 3], T[r_], T[r_ + 1]); } while (0)
      MFMA_DONE(sc[8]);
      TMAX(sc[8], 0, 16);
      SB();
#pragma unroll
      for (int hh = 0; hh < 2; ++hh) {
        put_k(L, 2 * w + hh, lane, hk[hh][0], hk[hh][1]);
        put_v(L, 2 * w + hh, lane, hk[hh][2], hk[hh][3]);
      }
      __syncthreads();
      STAMP(4); STAMPC(27);
      if (layer == 0) CHECK(fw, fwp);
      const unsigned w1s = mlp ? W_W1T + (j * 112 + 21 * w) * 1024 : W_END, w1d = 21 * w * 1024, l16 = lane * 16;
      dmaN<3>(rws, l16, w1s, w1d);
      v4u kb[3][2];
      unsigned ka = L_K + w * 2048 + lane * 16;
      OPAQUE(ka);
#define KRD(i) do { kb[(i) % 3][0] = *(LAS v4u*)(L + ka + (i) * 2048); kb[(i) % 3][1] = *(LAS v4u*)(L + ka + (i) * 2048 + 1024); } while (0)
#define CLIP(i) do { if (__builtin_expect(TT - 8 + (i) < 0, 0)) { asm volatile("" ::: "memory"); _Pragma("unroll") for (int r_ = 0; r_ < 16; ++r_) sc[i][r_] = -1e30f; } } while (0)
      KRD(0); KRD(1); KRD(2);
      SB();
      mfma_vv(sc[0], kb[0][0], qf[0]); mfma_vv(sc[0], kb[0][1], qf[1]);
#pragma unroll
      for (int i = 1; i < 8; ++i) {
        mfma_v0(sc[i], kb[i % 3][0], qf[0]);
        asm volatile("s_nop 3" : "+v"(sc[i - 1]));
        CLIP(i - 1);
        TMAX(sc[i - 1], 0, 8);
        SB();
        mfma_vv(sc[i], kb[i % 3][1], qf[1]);
        if (i + 2 < 8) KRD(i + 2);
        TMAX(sc[i - 1], 8, 16);
        SB();
      }
      unsigned vb = L_V + w * 2048 + tp0;
      OPAQUE(vb);
      v4u vf[3];
      vf[0] = trf(L, vb, vb + 512);
      vf[1] = trf(L, vb + 1024, vb + 1536);
      MFMA_DONE(sc[7]);
      CLIP(7);
      TMAX(sc[7], 0, 16);
      float m = max3(max3(mx[0], mx[1], mx[2]), mx[3], mx[3]);
      m = max3(m, xh(m), m);
      const f32x2 nm = {-m, -m};
      f32x16 o;
#pragma unroll
      for (int st = 0; st < 18; ++st) {
        f32x2 e[4];
#pragma unroll
        for (int d = 0; d < 4; ++d) e[d] = f32x2{sc[st >> 1][8 * (st & 1) + 2 * d], sc[st >> 1][8 * (st & 1) + 2 * d + 1]} + nm;
        v4u pf;
        SB();
#pragma unroll
        for (int d = 0; d < 4; ++d) { e[d][0] = __builtin_amdgcn_exp2f(e[d][0]); e[d][1] = __builtin_amdgcn_exp2f(e[d][1]); }
        SB();
#pragma unroll
        for (int d = 0; d < 4; ++d) pf[d] = pk(e[d]);
        SB();
        if (st + 2 < 18) vf[(st + 2) % 3] = trf(L, vb + (st + 2) * 1024, vb + (st + 2) * 1024 + 512);
        else asm volatile("s_nop 1" : "+v"(pf));
        if (st % 2 == 0 && st < 12) dmaN<3>(rws, l16, w1s + (3 * (st / 2) + 3) * 1024, w1d + (3 * (st / 2) + 3) * 1024);
        if (st % 2 == 1 && st < 16 && layer == 0 && mlp) W2PART(st / 2);
        SB();
        if (st == 0) mfma_v0(o, vf[0], pf); else mfma_vv(o, vf[st % 3], pf);
        SB();
      }
      MFMA_DONE(o);
      const float other = xh(o[12]);
      const float inv = __builtin_amdgcn_rcpf(h ? o[12] : other);
#pragma unroll
      for (int q = 0; q < 4; ++q) ST8((v2u{pk(f32x2{o[4 * q], o[4 * q + 1]} * inv), pk(f32x2{o[4 * q + 2], o[4 * q + 3]} * inv)}), oaddr[q]);
      STAMP(20); STAMPC(28);
      VMWAIT(0);
      __syncthreads();
      STAMP(5);
      if (w == 0 && lane < 14) __hip_atomic_store(flag_line(ws, layer, E_O, c * 16 + lane) + j, epoch, RLX);
    }
    if (mlp) {
      FRESH();
      dmaN<4>(rws, lane * 16, W_W1T + (j * 112 + 84 + 7 * w) * 1024, (84 + 7 * w) * 1024);
      dmaN<3>(rws, lane * 16, W_W1T + (j * 112 + 88 + 7 * w) * 1024, (88 + 7 * w) * 1024);
      v2u res[4];
#pragma unroll
      for (int q = 0; q < 4; ++q) res[q] = LD8(W_X + layer * X_L + TT * XB + 2 * j * 1024 + n * 16 + 8 * h + (q >> 1) * 1024 + (q & 1) * 512);
      if (w == 0) poll(flag_line(ws, layer, E_O, wg) + (lane & 15), epoch, dead);
      __syncthreads();
      STAMP(6);
      FRESH();
      {
        f32x16 a[1];
        kloop<1, 512>(L, rws, W_O + layer * X_L + TT * XB + lane * 16, tp0 + L_WO, 0u, a, []() {});
        STAMP(7);
        MFMA_DONE(a[0]);
        unsigned o[8];
#pragma unroll
        for (int q = 0; q < 4; ++q) {
          o[2 * q] = pk(f32x2{a[0][4 * q], a[0][4 * q + 1]} + unpk(res[q].x));
          o[2 * q + 1] = pk(f32x2{a[0][4 * q + 2], a[0][4 * q + 3]} + unpk(res[q].y));
        }
        const int xo = W_X1 + layer * X_L + TT * XB;
        ST16((v4u{o[0], o[1], o[2], o[3]}), xo + 2 * j * 1024 + lane * 16);
        ST16((v4u{o[4], o[5], o[6], o[7]}), xo + 2 * j * 1024 + 1024 + lane * 16);
        float s = 0.f, q2 = 0.f;
#pragma unroll
        for (int e = 0; e < 8; ++e) { s = dot2(o[e], ONES, s); q2 = dot2(o[e], o[e], q2); }
        s += xh(s); q2 += xh(q2);
        ST8((v2u{__builtin_bit_cast(unsigned, s), __builtin_bit_cast(unsigned, q2)}), h ? W_END : xo + X_ST + j * 256 + n * 8);
      }
      VMWAIT(0);
      __syncthreads();
      STAMP(8);
      if (w == 0 && lane < 14) __hip_atomic_store(flag_line(ws, layer, E_X1, c * 16 + lane) + j, epoch, RLX);
      if (layer == 0) {
        w2range<24, 32>(w2f, rws, W2V);
        f32x16 ca = zero16();
        colsum<0>(L, w * 1024 + lane * 16, 4, ca);
        colsum_put(L, ca, M_CS1 + w * 128, lane);
      }
      if (w == 0) poll(flag_line(ws, layer, E_X1, wg) + (lane < 14 ? lane : 0), epoch, dead);
      __syncthreads();
      STAMP(9); STAMPC(24);
      FRESH();
      {
        v2u st[7];
#pragma unroll
        for (int i = 0; i < 7; ++i) st[i] = LD8(W_X1 + layer * X_L + TT * XB + X_ST + (7 * h + i) * 256 + n * 8);
        SB();
        f32x16 a[4];
        kloop<4, 0>(L, rws, W_X1 + layer * X_L + TT * XB + lane * 16, lane * 16, 0u, a, []() {});
        STAMP(10); STAMPC(25);
        if (layer == 0) __builtin_amdgcn_s_barrier();
        f32x4 cs[4], bb1[4];
#pragma unroll
        for (int q = 0; q < 4; ++q) { cs[q] = *(LAS f32x4*)(L + M_CS1 + q * 32 + h * 16); bb1[q] = *(LAS f32x4*)(L + M_B1 + q * 32 + h * 16); }
        row_stats(st, mean, var);
        float rs = __builtin_amdgcn_rsqf(var + 1e-5f);
        rs = rs * __builtin_amdgcn_rsqf(var * rs * rs + 1e-5f);
        const float nmr = -mean * rs;
        asm volatile("s_nop 7" : "+v"(a[0]), "+v"(a[1]), "+v"(a[2]), "+v"(a[3]));
#pragma unroll
        for (int cc = 0; cc < 4; ++cc) {
          unsigned o[8];
          f32x2 v[8], z[8];
#pragma unroll
          for (int e = 0; e < 8; ++e) z[e] = f32x2{cs[e >> 1][2 * (e & 1)], cs[e >> 1][2 * (e & 1) + 1]} * nmr + f32x2{bb1[e >> 1][2 * (e & 1)], bb1[e >> 1][2 * (e & 1) + 1]};
          SB();
          if (cc < 3) {
#pragma unroll
            for (int q = 0; q < 4; ++q) { cs[q] = *(LAS f32x4*)(L + M_CS1 + (cc + 1) * 128 + q * 32 + h * 16); bb1[q] = *(LAS f32x4*)(L + M_B1 + (cc + 1) * 128 + q * 32 + h * 16); }
          }
          if (layer == 0) { if (cc == 0) w2range<32, 38>(w2f, rws, W2V); if (cc == 1) w2range<38, 44>(w2f, rws, W2V); if (cc == 2) w2range<44, 50>(w2f, rws, W2V); if (cc == 3) w2range<50, 56>(w2f, rws, W2V); }
#pragma unroll
          for (int e = 0; e < 8; ++e) v[e] = f32x2{a[cc][2 * e], a[cc][2 * e + 1]} * rs + z[e];
          SB();
#pragma unroll
          for (int e = 0; e < 8; ++e) z[e] = v[e] * v[e];
          SB();
#pragma unroll
          for (int e = 0; e < 8; ++e) z[e] = z[e] * -0.10294324f - 2.3022082f;
          SB();
#pragma unroll
          for (int e = 0; e < 8; ++e) z[e] *= v[e];
          SB();
#pragma unroll
          for (int e = 0; e < 8; ++e) { z[e][0] = __builtin_amdgcn_exp2f(z[e][0]); z[e][1] = __builtin_amdgcn_exp2f(z[e][1]); }
          SB();
#pragma unroll
          for (int e = 0; e < 8; ++e) z[e] += 1.0f;
          if (layer == 0) { if (cc < 3) dmaN<4>(rws, lane * 16, W_WQT + j * 3 * U + (14 * w + 4 * cc) * 1024, (14 * w + 4 * cc) * 1024); else dmaN<2>(rws, lane * 16, W_WQT + j * 3 * U + (14 * w + 12) * 1024, (14 * w + 12) * 1024); }
          SB();
#pragma unroll
          for (int e = 0; e < 8; ++e) { z[e][0] = __builtin_amdgcn_rcpf(z[e][0]); z[e][1] = __builtin_amdgcn_rcpf(z[e][1]); }
          SB();
#pragma unroll
          for (int e = 0; e < 8; ++e) v[e] *= z[e];
          SB();
#pragma unroll
          for (int e = 0; e < 8; ++e) o[e] = pk(v[e]);
          const int ho = W_H + layer * H_L + TT * HB + (8 * j + 2 * cc) * 1024 + lane * 16;
          ST16((v4u{o[0], o[1], o[2], o[3]}), ho);
          ST16((v4u{o[4], o[5], o[6], o[7]}), ho + 1024);
          SB();
          if (cc == 2) {
            if (layer == 0) VMWAIT(12); else VMWAIT(2);
            __builtin_amdgcn_s_barrier();
            if (w == 0 && lane < 14) __hip_atomic_store(flag_line(ws, layer, E_H0, c * 16 + lane) + j, epoch, RLX);
          }
        }
      }
      STAMP(21); STAMPC(26);
      VMWAIT(0);
      __syncthreads();
      STAMP(11);
      if (w == 0 && lane < 14) __hip_atomic_store(flag_line(ws, layer, E_H1, c * 16 + lane) + j, epoch, RLX);
      FRESH();
      const int T5 = 4 * c + 2 * hf5, k5 = 4 * jb + w;
      v2u r5[2][2];
#pragma unroll
      for (int m = 0; m < 2; ++m)
#pragma unroll
        for (int e = 0; e < 2; ++e) r5[m][e] = LD8(W_X1 + layer * X_L + (T5 + m) * XB + k5 * 1024 + e * 512 + n * 16 + 8 * h);
      const v4u bb2 = *(const v4u*)(b2 + k5 * 16 + 8 * h);
      if (w == 0) poll(flag_line(ws, layer, E_H0, wg) + (lane < 14 ? lane : 0), epoch, dead);
      __syncthreads();
      STAMP(12); STAMPC(29);
      {
        f32x16 acc[2][2];
        v4u hf[12];
        const unsigned hv = W_H + layer * H_L + T5 * HB + w * 1024 + lane * 16;
        auto ldh = [&](int i) {
          const int so = (i < 14 ? 8 * i : 8 * (i - 14) + 4) * 1024;
          hf[2 * (i % 6)] = __builtin_amdgcn_raw_buffer_load_b128(rws, hv, so, 16);
          hf[2 * (i % 6) + 1] = __builtin_amdgcn_raw_buffer_load_b128(rws, hv, so + HB, 16);
        };
        GAS u64* fp1 = flag_line(ws, layer, E_H1, wg) + (lane < 14 ? lane : 0);
        u64 f1;
#pragma unroll
        for (int i = 0; i < 5; ++i) ldh(i);
        SB();
#pragma unroll
        for (int i = 0; i < 28; ++i) {
#pragma unroll
          for (int m = 0; m < 2; ++m) {
#pragma unroll
            for (int x = 0; x < 2; ++x) { if (i == 0) mfma_a0(acc[x][m], w2f[i][x], hf[m]); else mfma_av(acc[x][m], w2f[i][x], hf[2 * (i % 6) + m]); }
            SB();
            if (m == 0 && i == 4) f1 = __hip_atomic_load(fp1, RLX);
            if (m == 0 && i == 9) CHECK(f1, fp1);
            if (m == 0 && i + 5 < 28) ldh(i + 5);
            SB();
          }
        }
        STAMP(13); STAMPC(30);
        FRESH();
        asm volatile("s_nop 11" : "+v"(acc[0][0]), "+v"(acc[0][1]), "+v"(acc[1][0]), "+v"(acc[1][1]));
#pragma unroll
        for (int d = 0; d < 4; ++d)
#pragma unroll
          for (int x = 0; x < 2; ++x)
#pragma unroll
            for (int m = 0; m < 2; ++m)
              *(LAS f32x4*)(L + L_RED + w * 4096 + lane * 16 + d * 16384 + (x * 2 + m) * 1024) = f32x4{acc[x][m][4 * d], acc[x][m][4 * d + 1], acc[x][m][4 * d + 2], acc[x][m][4 * d + 3]};
        __syncthreads();
        f32x4 p[2][2];
#pragma unroll
        for (int s = 0; s < 4; ++s)
#pragma unroll
          for (int x = 0; x < 2; ++x)
#pragma unroll
            for (int m = 0; m < 2; ++m) { const f32x4 t = *(LAS f32x4*)(L + L_RED + w * 16384 + lane * 16 + s * 4096 + (x * 2 + m) * 1024); p[x][m] = s == 0 ? t : p[x][m] + t; }
#pragma unroll
        for (int m = 0; m < 2; ++m) {
          const unsigned rr[4] = {r5[m][0].x, r5[m][0].y, r5[m][1].x, r5[m][1].y};
          v4u o;
#pragma unroll
          for (int d = 0; d < 4; ++d) o[d] = pk(f32x2{p[d >> 1][m][2 * (d & 1)], p[d >> 1][m][2 * (d & 1) + 1]} + unpk(rr[d]) + unpk(bb2[d]));
          if (layer == 0) {
            ST16(o, W_X + X_L + (4 * c + 2 * hf5 + m) * XB + (4 * jb + w) * 1024 + lane * 16);
            float s = 0.f, q2 = 0.f;
#pragma unroll
            for (int e = 0; e < 4; ++e) { s = dot2(o[e], ONES, s); q2 = dot2(o[e], o[e], q2); }
            *(LAS f32x2*)(L + L_RST + ((m * 8 + w * 2 + h) * 32 + n) * 8) = f32x2{s, q2};
          } else {
            *(v4u*)(out + (32 * (4 * c + 2 * hf5 + m) + n) * 448 + (4 * jb + w) * 16 + 8 * h) = o;
          }
        }
      }
    } else if (layer == 1 && wg == 15 && w == 0) {
      bool ok = false;
      for (int spins = 0; spins < SPIN_CAP && !ok; ++spins) {
        bool a = true;
#pragma unroll
        for (int i = 0; i < 4; ++i) a &= __hip_atomic_load((GAS u64*)(ws + W_STARTED) + lane + 64 * i, RLX) == epoch;
        ok = __builtin_amdgcn_ballot_w64(!a) == 0;
      }
      if (lane == 0) __hip_atomic_store((GAS u64*)(ws + W_E), epoch, RLX);
    }
    if (layer == 0) {
      FRESH();
      __syncthreads();
      if (mlp && w < 2) {
        f32x2 t = {0.f, 0.f};
#pragma unroll
        for (int i = 0; i < 4; ++i) t += *(LAS f32x2*)(L + L_RST + ((h * 8 + 4 * w + i) * 32 + n) * 8);
        ST8(__builtin_bit_cast(v2u, t), W_X + X_L + (4 * c + 2 * hf5 + h) * XB + X_ST + (2 * jb + w) * 256 + n * 8);
      }
      VMWAIT(0);
      __syncthreads();
      STAMP(14);
      if (w == 0) {
        if (mlp && lane < 16) __hip_atomic_store(flag_line(ws, 1, E_X, c * 16 + lane) + j, epoch, RLX);
        poll(flag_line(ws, 1, E_X, wg) + (lane < 14 ? lane : 0), epoch, dead);
      } else {
        if (mlp) copy_in(rws, lane, W_WQT + j * 3 * U + (56 + 10 * (w - 1)) * 1024, (56 + 10 * (w - 1)) * 1024, w == 3 ? 8 : 10);
        else copy_in(rws, lane, W_WQT + (j * 3 + w - 1) * U, (w - 1) * U, 28);
        VMWAIT(0);
      }
      __syncthreads();
      STAMP(15);
    }
    STAMP(16);
  }
}
}

extern "C" void kernel_launch(void* const* d_in, const int* in_sizes, int n_in, void* d_out, int out_size, void* d_ws, size_t ws_size, hipStream_t stream) {
  (void)hipFuncSetAttribute((const void*)mk::fused, hipFuncAttributeMaxDynamicSharedMemorySize, mk::L_TOTAL);
  mk::fused<<<256, 256, mk::L_TOTAL, stream>>>((const unsigned short*)d_in[0], (const unsigned short*)d_in[1], (const unsigned short*)d_in[2],
      (const unsigned short*)d_in[4], (const unsigned short*)d_in[5], (const unsigned short*)d_in[6], (const unsigned short*)d_in[7],
      (unsigned short*)d_out, (unsigned char*)d_ws);
}
```

```cpp
#include <hip/hip_runtime.h>
#include <stdint.h>

namespace mk {
typedef unsigned v4u __attribute__((ext_vector_type(4)));
typedef unsigned v2u __attribute__((ext_vector_type(2)));
typedef float f32x4 __attribute__((ext_vector_type(4)));
typedef float f32x2 __attribute__((ext_vector_type(2)));
typedef float f32x16 __attribute__((ext_vector_type(16)));
typedef short s16x4 __attribute__((ext_vector_type(4)));
typedef __bf16 bf16x8 __attribute__((ext_vector_type(8)));
typedef __bf16 bf16x2 __attribute__((ext_vector_type(2)));
typedef unsigned long long u64;
typedef __amdgpu_buffer_rsrc_t rsrc_t;
#define LAS __attribute__((address_space(3)))
#define GAS __attribute__((address_space(1)))
#define RLX __ATOMIC_RELAXED, __HIP_MEMORY_SCOPE_AGENT
#define DI __device__ __forceinline__
#define SB() __builtin_amdgcn_sched_barrier(0)
#define OPAQUE(x) asm volatile("" : "+v"(x))

constexpr int SEQ = 2048, DM = 448, FF = 1792;
constexpr int U = 28672;
constexpr int L_K = 3 * U, L_V = L_K + 22 * 1024, L_WO = L_K + 46 * 1024, L_MISC = L_WO + U, L_TOTAL = 163840;
constexpr int L_RED = 2 * U, L_RST = L_RED + 65536;
constexpr int M_CSQ = L_MISC, M_CS1 = L_MISC + 384, M_B1 = L_MISC + 896, M_ST = L_MISC + 1408;
constexpr int L_XS = 2 * U;
constexpr int L_CSP = L_V;
static_assert(L_MISC + 2048 == L_TOTAL && L_XS + 4 * 24576 <= L_MISC, "LDS map");
constexpr int W_E = 0, W_STARTED = 4096, W_FLAG = 8192;
enum { E_KV = 0  , E_O = 4, E_X1 = 5, E_H0 = 6, E_H1 = 7, E_X = 8, E_W2 = 9  , E_W1 = 11, E_WQ = 12, NEDGE = 13 };
constexpr int W_KV = 1 << 20, KV_L = 16 * 64 * 4096;
constexpr int XB = 32 * 1024, X_L = 64 * XB;
constexpr int X_ST = 28 * 1024;
constexpr int W_O = W_KV + 2 * KV_L, W_X1 = W_O + X_L, W_X = W_X1 + X_L, W_H = W_X + X_L;
constexpr int HB = 112 * 1024, H_L = 64 * HB;
constexpr int W_W2T = W_H + H_L;
constexpr int W_W1T = W_W2T + 7 * 112 * 2048;
constexpr int W_WQT = W_W1T + 14 * 112 * 1024;
constexpr int W_WOT = W_WQT + 16 * 3 * U;
constexpr int W_END = W_WOT + 14 * U;
constexpr int W_STAMP = W_END;
static_assert(W_FLAG + 2 * 256 * NEDGE * 128 <= W_KV, "flags");
constexpr int SPIN_CAP = 1 << 16;
constexpr int CODE_LINES = 236;
constexpr unsigned ONES = 0x3f803f80u;

DI f32x16 mfma(v4u a, v4u b, f32x16 c) {
  return __builtin_amdgcn_mfma_f32_32x32x16_bf16(__builtin_bit_cast(bf16x8, a), __builtin_bit_cast(bf16x8, b), c, 0, 0, 0);
}
DI void mfma_v0(f32x16& acc, v4u a, v4u b) { asm volatile("v_mfma_f32_32x32x16_bf16 %0, %1, %2, 0" : "=&v"(acc) : "v"(a), "v"(b)); }
DI void mfma_vv(f32x16& acc, v4u a, v4u b) { asm volatile("v_mfma_f32_32x32x16_bf16 %0, %1, %2, %0" : "+v"(acc) : "v"(a), "v"(b)); }
DI void mfma_a0(f32x16& acc, v4u a, v4u b) { asm volatile("v_mfma_f32_32x32x16_bf16 %0, %1, %2, 0" : "=&v"(acc) : "a"(a), "v"(b)); }
DI void mfma_av(f32x16& acc, v4u a, v4u b) { asm volatile("v_mfma_f32_32x32x16_bf16 %0, %1, %2, %0" : "+v"(acc) : "a"(a), "v"(b)); }
#define MFMA_DONE(x) asm volatile("s_nop 11" : "+v"(x))
DI v2u trd(LAS unsigned char* L, unsigned a) {
  return __builtin_bit_cast(v2u, __builtin_amdgcn_ds_read_tr16_b64_v4i16((LAS s16x4*)(L + a)));
}
DI v4u trf(LAS unsigned char* L, unsigned a0, unsigned a1) { const v2u p = trd(L, a0), q = trd(L, a1); return v4u{p.x, p.y, q.x, q.y}; }
DI unsigned pk(float a, float b) { f32x2 f = {a, b}; return __builtin_bit_cast(unsigned, __builtin_convertvector(f, bf16x2)); }
DI unsigned pk(f32x2 f) { return __builtin_bit_cast(unsigned, __builtin_convertvector(f, bf16x2)); }
DI float blo(unsigned u) { return __builtin_bit_cast(float, u << 16); }
DI float bhi(unsigned u) { return __builtin_bit_cast(float, u & 0xffff0000u); }
DI f32x2 unpk(unsigned u) { return f32x2{blo(u), bhi(u)}; }
DI int rfl(int x) { return __builtin_amdgcn_readfirstlane(x); }
DI float dot2(unsigned a, unsigned b, float c) { return __builtin_amdgcn_fdot2_f32_bf16(__builtin_bit_cast(bf16x2, a), __builtin_bit_cast(bf16x2, b), c, false); }
DI float max3(float a, float b, float c) { float r; asm("v_max3_f32 %0, %1, %2, %3" : "=v"(r) : "v"(a), "v"(b), "v"(c)); return r; }
DI float xh(float v) {
  const unsigned u = __builtin_bit_cast(unsigned, v);
  const auto r = __builtin_amdgcn_permlane32_swap(u, u, false, false);
  return __builtin_bit_cast(float, (threadIdx.x & 32) ? r[0] : r[1]);
}
DI rsrc_t mkrs(const void* p, int bytes) { return __builtin_amdgcn_make_buffer_rsrc((void*)p, 0, bytes, 0x00020000); }
DI void dma16(rsrc_t rs, unsigned voff, unsigned soff, unsigned ldst) {
  asm volatile("s_mov_b32 m0, %0\n\ts_nop 0\n\tbuffer_load_dwordx4 %1, %2, %3 offen lds" :: "s"(ldst), "v"(voff), "s"(rs), "s"(soff) : "memory");
}
DI void dma16c(rsrc_t rs, unsigned voff, unsigned soff, unsigned ldst) {
  asm volatile("s_mov_b32 m0, %0\n\ts_nop 0\n\tbuffer_load_dwordx4 %1, %2, %3 offen sc1 lds" :: "s"(ldst), "v"(voff), "s"(rs), "s"(soff) : "memory");
}
template <int N> DI void dmaN(rsrc_t rs, unsigned voff, unsigned soff, unsigned ldst) {
  if constexpr (N == 4)
    asm volatile("s_mov_b32 m0, %0\n\ts_nop 0\n\tbuffer_load_dwordx4 %1, %2, %3 offen sc1 lds\n\tbuffer_load_dwordx4 %1, %2, %3 offen offset:1024 sc1 lds\n\t"
                 "buffer_load_dwordx4 %1, %2, %3 offen offset:2048 sc1 lds\n\tbuffer_load_dwordx4 %1, %2, %3 offen offset:3072 sc1 lds" :: "s"(ldst), "v"(voff), "s"(rs), "s"(soff) : "memory");
  else if constexpr (N == 3)
    asm volatile("s_mov_b32 m0, %0\n\ts_nop 0\n\tbuffer_load_dwordx4 %1, %2, %3 offen sc1 lds\n\tbuffer_load_dwordx4 %1, %2, %3 offen offset:1024 sc1 lds\n\t"
                 "buffer_load_dwordx4 %1, %2, %3 offen offset:2048 sc1 lds" :: "s"(ldst), "v"(voff), "s"(rs), "s"(soff) : "memory");
  else
    asm volatile("s_mov_b32 m0, %0\n\ts_nop 0\n\tbuffer_load_dwordx4 %1, %2, %3 offen sc1 lds\n\tbuffer_load_dwordx4 %1, %2, %3 offen offset:1024 sc1 lds"
                 :: "s"(ldst), "v"(voff), "s"(rs), "s"(soff) : "memory");
}
DI void copy_in(rsrc_t rs, int lane, unsigned src, unsigned dst, int cnt) {
#pragma unroll 1
  for (int i = 0; i < cnt; ++i) dma16c(rs, lane * 16, src + i * 1024, dst + i * 1024);
}
#define METER 5
#define STR2(x) #x
#define STR(x) STR2(x)
#define VMWAIT(n) asm volatile("s_waitcnt vmcnt(" #n ")" ::: "memory")
#define ST16(v, off) __builtin_amdgcn_raw_buffer_store_b128(v, rws, off, 0, 16)
#define ST8(v, off) __builtin_amdgcn_raw_buffer_store_b64(v, rws, off, 0, 16)
#define LD8(off) __builtin_amdgcn_raw_buffer_load_b64(rws, off, 0, 16)
DI unsigned tr_base(int lane, int tp, bool nat, int par) {
  const int h = lane >> 5, cb = (lane >> 4) & 1, q = (lane >> 2) & 3, p = lane & 3;
  const int row = nat ? 8 * h + 4 * tp + q : 8 * tp + 4 * h + q;
  return row * 64 + ((4 * cb + p + par) & 7) * 8;
}
DI void xwait6(v4u& x) { asm volatile("s_waitcnt vmcnt(6)" : "+v"(x) :: "memory"); }
template <int I> DI void xload(v4u& x, rsrc_t rs, unsigned voff, unsigned s0, unsigned s1) {
  asm volatile("buffer_load_dwordx4 %0, %1, %2, %3 offen offset:%4 sc1" : "=v"(x) : "v"(voff), "s"(rs), "s"(I < 4 ? s0 : s1), "i"((I & 3) * 1024) : "memory");
}
template <int I> DI void w2load(v4u (&w2f)[28][2], rsrc_t rs, unsigned voff) {
  asm volatile("buffer_load_dwordx4 %0, %1, %2, %3 offen offset:%4 sc1" : "=a"(w2f[I >> 1][I & 1]) : "v"(voff), "s"(rs),
               "s"(((I >> 1) < 14 ? 8 * (I >> 1) : 8 * ((I >> 1) - 14) + 4) * 2048), "i"((I & 1) * 1024) : "memory");
}
template <int LO, int HI> DI void w2range(v4u (&w2f)[28][2], rsrc_t rs, unsigned voff) {
  if constexpr (LO < HI) { w2load<LO>(w2f, rs, voff); w2range<LO + 1, HI>(w2f, rs, voff); }
}
template <int TR1> DI v4u wfrag(LAS unsigned char* L, unsigned b01, unsigned b2, int s, int q) {
  if constexpr (TR1 == 0) return *(LAS v4u*)(L + b01 + (s * 4 + q) * 1024);
  else { const unsigned a = q < 2 ? b01 + q * U + s * 1024 : b2 + s * 1024; return trf(L, a, a + TR1); }
}
template <int NC, int TR1, int S> DI void kstep(LAS unsigned char* L, v4u (&xr)[7], rsrc_t rs, unsigned voff, unsigned s0, unsigned s1, unsigned b01, unsigned b2,
                                                v4u (&wb)[NC == 1 ? 7 : NC], f32x16 (&acc)[NC]) {
  if constexpr (S < 7) {
    xwait6(xr[S]);
    SB();
#pragma unroll
    for (int q = 0; q < NC; ++q) {
      mfma_vv(acc[q], wb[NC == 1 ? S : q], xr[S]);
      SB();
      wb[NC == 1 ? S : q] = wfrag<TR1>(L, b01, b2, S + (NC == 1 ? 7 : 1), q);
      if (q == NC - 1) xload<S>(xr[S], rs, voff, s0, s1);
      SB();
    }
    kstep<NC, TR1, S + 1>(L, xr, rs, voff, s0, s1, b01, b2, wb, acc);
  }
}
template <int I> DI void xfirst(v4u (&xr)[7], rsrc_t rs, unsigned voff) { if constexpr (I < 7) { xload<I>(xr[I], rs, voff, 0u, 4096u); xfirst<I + 1>(xr, rs, voff); } }
template <int NC, int TR1> DI void kloop(LAS unsigned char* L, rsrc_t rs, unsigned voff, unsigned b01, unsigned b2, f32x16 (&acc)[NC]) {
  v4u xr[7], wb[NC == 1 ? 7 : NC];
  xfirst<0>(xr, rs, voff);
  OPAQUE(b01); OPAQUE(b2);
#pragma unroll
  for (int i = 0; i < (NC == 1 ? 7 : NC); ++i) wb[i] = wfrag<TR1>(L, b01, b2, NC == 1 ? i : 0, NC == 1 ? 0 : i);
#pragma unroll
  for (int q = 0; q < NC; ++q) {
#pragma unroll
    for (int r = 0; r < 16; ++r) acc[q][r] = 0.f;
    asm volatile("" : "+v"(acc[q]));
  }
#pragma unroll 1
  for (int it = 1; it <= 4; ++it) {
    const unsigned s0 = it < 4 ? it * 7168u : (unsigned)W_END;
    kstep<NC, TR1, 0>(L, xr, rs, voff, s0, s0 + 4096u, b01, b2, wb, acc);
    b01 += TR1 == 0 ? 7 * 4096 : 7 * 1024; b2 += 7 * 1024;
  }
  asm volatile("s_waitcnt vmcnt(0)" : "+v"(xr[0]), "+v"(xr[1]), "+v"(xr[2]), "+v"(xr[3]), "+v"(xr[4]), "+v"(xr[5]), "+v"(xr[6]) :: "memory");
}
template <int NC, class F> DI void kregs(LAS unsigned char* L, const v4u (&xf)[28], unsigned b, f32x16* acc, F bg) {
  constexpr int PD = 4 / NC;
  v4u wb[PD][NC];
  OPAQUE(b);
#pragma unroll
  for (int s = 0; s < PD; ++s)
#pragma unroll
    for (int q = 0; q < NC; ++q) wb[s][q] = trf(L, b + q * U + s * 1024, b + q * U + s * 1024 + 256);
#pragma unroll
  for (int s = 0; s < 28; ++s)
#pragma unroll
    for (int q = 0; q < NC; ++q) {
      SB();
      if (s == 0) mfma_v0(acc[q], wb[0][q], xf[0]); else mfma_vv(acc[q], wb[s % PD][q], xf[s]);
      SB();
      if (s + PD < 28) wb[s % PD][q] = trf(L, b + q * U + (s + PD) * 1024, b + q * U + (s + PD) * 1024 + 256);
      if (q == 0) bg(s);
    }
}
template <int TR1> DI void colsum(LAS unsigned char* L, unsigned b, int rounds, f32x16& a) {
  const v4u ones = {ONES, ONES, ONES, ONES};
#pragma unroll 1
  for (int s = 0; s < rounds; ++s) {
    OPAQUE(b);
    v4u f[7];
#pragma unroll
    for (int i = 0; i < 7; ++i) f[i] = wfrag<TR1>(L, b, b, i, 0);
    SB();
#pragma unroll
    for (int i = 0; i < 7; ++i) a = mfma(f[i], ones, a);
    b += TR1 == 0 ? 7 * 4096 : 7 * 1024;
  }
}
DI void colsum_put(LAS unsigned char* L, const f32x16& a, unsigned dst, int lane) {
  if ((lane & 31) == 0) {
#pragma unroll
    for (int q = 0; q < 4; ++q) *(LAS f32x4*)(L + dst + q * 32 + (lane >> 5) * 16) = f32x4{a[4 * q], a[4 * q + 1], a[4 * q + 2], a[4 * q + 3]};
  }
}
DI f32x16 zero16() { f32x16 a;
#pragma unroll
  for (int r = 0; r < 16; ++r) a[r] = 0.f;
  return a; }
DI GAS u64* flag_line(unsigned char* ws, int layer, int edge, int wg) { return (GAS u64*)(ws + W_FLAG + ((layer * 256 + wg) * NEDGE + edge) * 128); }
DI void poll(GAS u64* p, u64 epoch, bool& dead) {
  if (dead) return;
  for (int spins = 0;; ++spins) {
    const u64 f = __hip_atomic_load(p, RLX);
    if (__builtin_amdgcn_ballot_w64(f != epoch) == 0) break;
    if (spins > SPIN_CAP) { dead = true; break; }
  }
  __builtin_amdgcn_fence(__ATOMIC_ACQUIRE, "wavefront");
}
#define CHECK(f, p) do { if (__builtin_amdgcn_ballot_w64((f) != epoch) != 0) poll(p, epoch, dead); __builtin_amdgcn_fence(__ATOMIC_ACQUIRE, "wavefront"); } while (0)
#define STAMP(i)
#define STAMPC(i)
DI void put_k(LAS unsigned char* L, int lt, int lane, v4u a, v4u b) {
  *(LAS v4u*)(L + L_K + lt * 2048 + lane * 16) = a;
  *(LAS v4u*)(L + L_K + lt * 2048 + 1024 + lane * 16) = b;
}
DI void put_v(LAS unsigned char* L, int lt, int lane, v4u a, v4u b) {
  const unsigned o = L_V + lt * 2048 + (lane & 31) * 64 + (lane >> 5) * 8;
  *(LAS v2u*)(L + o) = v2u{a.x, a.y};
  *(LAS v2u*)(L + o + 16) = v2u{a.z, a.w};
  *(LAS v2u*)(L + o + 32) = v2u{b.x, b.y};
  *(LAS v2u*)(L + o + 48) = v2u{b.z, b.w};
}
DI void ln_pack(LAS unsigned char* L, const f32x16& a, unsigned cs, int h, float nmsc, float sc, float pad0, v4u& o0, v4u& o1) {
  unsigned o[8];
  f32x4 cq[4];
  f32x2 t[8];
#pragma unroll
  for (int q = 0; q < 4; ++q) cq[q] = *(LAS f32x4*)(L + cs + q * 32 + h * 16);
#pragma unroll
  for (int e = 0; e < 8; ++e) t[e] = f32x2{a[2 * e], a[2 * e + 1]} * sc;
  SB();
#pragma unroll
  for (int e = 0; e < 8; ++e) t[e] += f32x2{cq[e >> 1][2 * (e & 1)], cq[e >> 1][2 * (e & 1) + 1]} * nmsc;
  SB();
#pragma unroll
  for (int e = 0; e < 8; ++e) o[e] = pk(t[e]);
  if (h == 1) { o[6] = pk(pad0, 0.f); o[7] = 0u; }
  o0 = v4u{o[0], o[1], o[2], o[3]}; o1 = v4u{o[4], o[5], o[6], o[7]};
}
DI void row_stats(const v2u (&st)[7], float& mean, float& var) {
  f32x2 t = __builtin_bit_cast(f32x2, st[0]);
#pragma unroll
  for (int i = 1; i < 7; ++i) t += __builtin_bit_cast(f32x2, st[i]);
  mean = (t[0] + xh(t[0])) * (1.0f / DM);
  var = fmaxf((t[1] + xh(t[1])) * (1.0f / DM) - mean * mean, 0.f);
}

__global__ void __launch_bounds__(256, 1) fused(const unsigned short* __restrict__ xin, const unsigned short* __restrict__ wqkv, const unsigned short* __restrict__ wo,
    const unsigned short* __restrict__ w1, const unsigned short* __restrict__ b1, const unsigned short* __restrict__ w2, const unsigned short* __restrict__ b2,
    unsigned short* __restrict__ out, unsigned char* ws) {
  extern __shared__ __attribute__((aligned(16))) unsigned char smem[];
  LAS unsigned char* L = (LAS unsigned char*)smem;
  int lane = threadIdx.x & 63, w = rfl(threadIdx.x >> 6), n = lane & 31, h = lane >> 5;
  int c = 2 * (blockIdx.x & 7) + (blockIdx.x >> 7), j = (blockIdx.x >> 3) & 15;
  const bool mlp = j < 14;
  const int c0_ = c, j0_ = j, w0_ = w, lane0_ = lane;
#define wg (c * 16 + j)
#define TT (4 * c + w)
#define hf5 (j >= 7 ? 1 : 0)
#define jb (j >= 7 ? j - 7 : j)
#define W2V (W_W2T + (jb * 112 + w) * 2048 + lane * 16)
#define W2PART(i) do { if ((i) == 0) w2range<0, 4>(w2f, rws, W2V); if ((i) == 1) w2range<4, 8>(w2f, rws, W2V); if ((i) == 2) w2range<8, 12>(w2f, rws, W2V); \
    if ((i) == 3) w2range<12, 16>(w2f, rws, W2V); if ((i) == 4) w2range<16, 20>(w2f, rws, W2V); } while (0)
#define FRESH() do { c = c0_; j = j0_; w = w0_; lane = lane0_; asm volatile("" : "+s"(c), "+s"(j), "+s"(w)); asm volatile("" : "+v"(lane)); n = lane & 31; h = lane >> 5; } while (0)
  const rsrc_t rx = mkrs(xin, SEQ * DM * 2), rq = mkrs(wqkv, DM * 3 * DM * 2);
  STAMP(0);
  unsigned ctouch;
  {
    const unsigned char* pc = (const unsigned char*)__builtin_amdgcn_s_getpc();
    const int part = (j >> 2) + 4 * (c & 1);
    asm volatile("global_load_dword %0, %1, off" : "=v"(ctouch) : "v"(pc + ((w == (j & 3) && part * 64 + lane < CODE_LINES) ? part * 8192 + lane * 128 : 0)) : "memory");
  }
  v4u xf[28];
  {
    const unsigned vo = (32 * TT + (lane >> 3)) * 896;
    int cb = j >= 12 ? j - 12 : (j >= 6 ? j - 6 : j);
#pragma unroll 1
    for (int t = 0; t < 6; ++t) {
#pragma unroll
      for (int r = 0; r < 4; ++r) dma16(rx, vo + (((lane & 7) ^ (((lane >> 4) + 4 * r) & 7)) * 16), cb * 128 + r * 8 * 896, L_XS + w * 24576 + cb * 4096 + r * 1024);
      cb = cb == 5 ? 0 : cb + 1;
    }
#pragma unroll
    for (int i = 24; i < 28; ++i) asm volatile("buffer_load_dwordx4 %0, %1, %2, 0 offen offset:%3" : "=v"(xf[i]) : "v"((32 * TT + n) * 896 + 16 * h), "s"(rx), "i"(32 * i) : "memory");
    const unsigned vq = (lane >> 2) * 2688 + (lane & 3) * 16;
    const int i0 = 7 * ((w + 2 * (c & 1)) & 3);
#pragma unroll
    for (int i = 0; i < 14; ++i) dma16(rq, vq, (i0 + (i >> 1)) * 16 * 2688 + 896 + (i & 1) * 896 + ((56 * j) & ~15), (i & 1) * U + (i0 + (i >> 1)) * 1024);
  }
  unsigned touch;
  {
    const unsigned char* p = ws + 4096 + (size_t)(lane & 15) * (2u << 20);
    if (lane >= 16) p = (const unsigned char*)w1 + (7 * c >> 2) * 16 * 3584;
    if (lane == 17) p = (const unsigned char*)w2 + 7 * c * 16 * 896;
    if (lane == 18) p = (const unsigned char*)wo + c * 16 * 896;
    if (lane == 19) p = (const unsigned char*)b1;
    if (lane == 20) p = (const unsigned char*)b2;
    if (lane == 21) p = (const unsigned char*)out + c * 128 * 896;
    asm volatile("global_load_dword %0, %1, off" : "=v"(touch) : "v"(p) : "memory");
  }
  const rsrc_t rws = mkrs(ws, W_END);
  int layer = 0;
  STAMP(31 - 0 * layer);
  const u64 epoch = *(const u64*)(ws + W_E) + 1;
  bool dead = false;
  v4u qf[2], kf[2];
  {
      float mean, var;
      f32x16 a[3];
      v4u wo0, wo1;
      const unsigned tq = tr_base(lane, 0, true, j & 1);
        asm volatile("s_waitcnt vmcnt(15)" : "+v"(xf[24]), "+v"(xf[25]), "+v"(xf[26]), "+v"(xf[27]) :: "memory");
        STAMP(22);
        const unsigned xb = L_XS + w * 24576 + n * 128;
#pragma unroll
        for (int i = 0; i < 24; ++i) xf[i] = *(LAS v4u*)(L + xb + (((2 * (i & 3) + h) ^ ((n >> 1) & 7)) * 16) + (i >> 2) * 4096);
        float s[2] = {0.f, 0.f}, q[2] = {0.f, 0.f};
#pragma unroll
        for (int i = 0; i < 28; ++i)
#pragma unroll
          for (int e = 0; e < 4; ++e) { s[e & 1] = dot2(xf[(i + 24) % 28][e], ONES, s[e & 1]); q[e & 1] = dot2(xf[(i + 24) % 28][e], xf[(i + 24) % 28][e], q[e & 1]); }
        mean = s[0] + s[1]; var = q[0] + q[1];
        mean = (mean + xh(mean)) * (1.0f / DM);
        var = fmaxf((var + xh(var)) * (1.0f / DM) - mean * mean, 0.f);
        STAMP(17);
        asm volatile("s_waitcnt vmcnt(0)" : "+v"(touch), "+v"(ctouch) :: "memory");
        __syncthreads();
        STAMP(18);
        const unsigned vq = (lane >> 2) * 2688 + (lane & 3) * 16;
        const int i0 = 7 * ((w + 2 * (c & 1)) & 3);
        const rsrc_t rw1 = mkrs(w1, mlp ? DM * FF * 2 : 0), rw2 = mkrs(w2, mlp ? FF * DM * 2 : 0), ro = mkrs(wo, mlp ? DM * DM * 2 : 0);
        const int so = ((7 * c + 4 * hf5 + w) * 16) * 896 + jb * 128, f0 = 7 * c + w, f1 = f0 + 4;
        auto bg = [&](int s) {
          if (s % 4 == 0) dma16(rq, vq, (i0 + s / 4) * 16 * 2688 + ((56 * j) & ~15), 2 * U + (i0 + s / 4) * 1024);
        };
        if (threadIdx.x == 0) __hip_atomic_store((GAS u64*)(ws + W_STARTED) + wg, epoch, RLX);
        SB();
        STAMP(1);
#pragma unroll 1
        for (int im = 0; im < 2; ++im) { f32x16 ca = zero16(); colsum<256>(L, tq + im * U + 7 * w * 1024, 1, ca); colsum_put(L, ca, L_CSP + (im * 4 + w) * 128, lane); }
        STAMP(14);
        kregs<2>(L, xf, tq, a, bg);
        STAMP(15);
        __syncthreads();
        if (lane < 16) {
          const unsigned o = (lane >> 3) * 512 + (lane & 7) * 16;
          *(LAS f32x4*)(L + M_CSQ + lane * 16) = (*(LAS f32x4*)(L + L_CSP + o) + *(LAS f32x4*)(L + L_CSP + o + 128)) + (*(LAS f32x4*)(L + L_CSP + o + 256) + *(LAS f32x4*)(L + L_CSP + o + 384));
        }
      STAMP(23);
      const float rs = __builtin_amdgcn_rsqf(var + 1e-5f);
      asm volatile("s_nop 7" : "+v"(a[0]), "+v"(a[1]));
      const int kvoff = W_KV + 0 * KV_L + (j * 64 + TT) * 4096 + lane * 16;
      v4u v0, v1;
      ln_pack(L, a[0], M_CSQ, h, -mean * rs, rs, 0.f, kf[0], kf[1]);
      ST16(kf[0], kvoff); ST16(kf[1], kvoff + 1024);
      ln_pack(L, a[1], M_CSQ + 128, h, -mean * rs, rs, 1.f, v0, v1);
      ST16(v0, kvoff + 2048); ST16(v1, kvoff + 3072);
      if (w < 3) put_k(L, 8 + w, lane, kf[0], kf[1]);
      put_v(L, 8 + w, lane, v0, v1);
      STAMP(19);
      {
        dma16(rw2, (lane >> 3) * 896 + (lane & 7) * 16, so, L_WO + w * 4096);
        dma16(rw2, (lane >> 3) * 896 + (lane & 7) * 16, so + 8 * 896, L_WO + w * 4096 + 1024);
        dma16(rw1, (lane >> 2) * 3584 + (lane & 3) * 16, (f0 >> 2) * 16 * 3584 + j * 256 + (f0 & 3) * 64, L_WO + w * 4096 + 2048);
        dma16(rw1, (lane >> 2) * 3584 + (lane & 3) * 16, (f1 >> 2) * 16 * 3584 + j * 256 + (f1 & 3) * 64, L_WO + w * 4096 + 3072);
        wo0 = __builtin_amdgcn_raw_buffer_load_b128(ro, (16 * (c + 16 * (w & 1)) + (lane >> 2)) * 896 + (lane & 3) * 16 + j * 64, 0, 0);
        wo1.x = __builtin_amdgcn_raw_buffer_load_b32(mkrs(b1, mlp ? FF * 2 : 0), 256 * j + 4 * lane, 0, 0);
        wo1.y = __builtin_amdgcn_raw_buffer_load_b32(rws, W_FLAG + (((lane >> 4) & 1) * 256 + wg) * NEDGE * 128 + (lane & 15) * 128, 0, 16);
        SB();
        VMWAIT(11);
        __syncthreads();
        STAMP(6);
        { f32x16 ca = zero16(); colsum<256>(L, tq + 2 * U + 7 * w * 1024, 1, ca); colsum_put(L, ca, L_CSP + (8 + w) * 128, lane); }
        VMWAIT(7);
        if (lane < 2 && c + 1 + lane < 16) __hip_atomic_store(flag_line(ws, 0, E_KV + (lane ? 0 : 2) + (w >> 1), wg + 16 * (1 + lane)) + (w & 1), epoch, RLX);
        STAMP(7);
        kregs<1>(L, xf, tq + 2 * U, a + 2, [](int) {});
        VMWAIT(1);
        asm volatile("" :: "v"(wo1.y));
        {
          const unsigned ta = L_WO + w * 4096 + (4 * h + ((lane >> 2) & 3)) * 128 + (8 * ((lane >> 4) & 1) + 4 * ((lane >> 1) & 1) + 2 * (lane & 1)) * 8;
          const int o2 = (mlp && w + hf5 < 4) ? W_W2T + (jb * 112 + 7 * c + 4 * hf5 + w) * 2048 + lane * 16 : W_END;
          ST16(trf(L, ta, ta + 1024), o2);
          ST16(trf(L, ta + 8, ta + 1032), o2 + 1024);
          const unsigned tb = L_WO + w * 4096 + 2048 + tr_base(lane, 0, false, 0);
          const int o1 = W_W1T + (j * 112 + 7 * c + w) * 1024 + lane * 16;
          ST16(trf(L, tb, tb + 512), mlp ? o1 : W_END);
          ST16(trf(L, tb + 1024, tb + 1536), (mlp && w < 3) ? o1 + 4096 : W_END);
          ST16(wo0, (mlp && w < 2 && c + 16 * w < 28) ? W_WOT + j * U + (c + 16 * w) * 1024 + lane * 16 : W_END);
          if (w < 3) {
            ST16(*(LAS v4u*)(L + w * U + c * 1024 + lane * 16), W_WQT + (j * 3 + w) * U + c * 1024 + lane * 16);
            if (c < 12) ST16(*(LAS v4u*)(L + w * U + (c + 16) * 1024 + lane * 16), W_WQT + (j * 3 + w) * U + (c + 16) * 1024 + lane * 16);
          } else {
            *(LAS f32x2*)(L + M_B1 + lane * 8) = unpk(wo1.x);
          }
        }
        __syncthreads();
        if (lane < 8) {
          const unsigned o = 1024 + lane * 16;
          *(LAS f32x4*)(L + M_CSQ + 256 + lane * 16) = (*(LAS f32x4*)(L + L_CSP + o) + *(LAS f32x4*)(L + L_CSP + o + 128)) + (*(LAS f32x4*)(L + L_CSP + o + 256) + *(LAS f32x4*)(L + L_CSP + o + 384));
        }
        MFMA_DONE(a[2]);
        STAMP(9);
      }
      const float sq = rs * (0.18898223650461362f * 1.4426950408889634f);
      ln_pack(L, a[2], M_CSQ + 256, h, -mean * sq, sq, 0.f, qf[0], qf[1]);
  }
  v4u w2f[28][2];
#pragma unroll
  for (int i = 0; i < 28; ++i) { asm volatile("" : "=a"(w2f[i][0])); asm volatile("" : "=a"(w2f[i][1])); }

  for (; layer < 2; ++layer) {
    FRESH();
    float mean, var;
    f32x16 sc[9];
    int oaddr[4];
    u64 fw;
    GAS u64* fwp = flag_line(ws, 0, E_W2, wg) + ((mlp || lane >= 48) ? lane : 48);
    {
      if (layer == 1) {
      STAMP(1);
      f32x16 a[3];
      const unsigned tq = tr_base(lane, 0, true, j & 1);
      v2u st[7];
#pragma unroll
      for (int i = 0; i < 7; ++i) st[i] = LD8(W_X + TT * XB + X_ST + (7 * h + i) * 256 + n * 8);
      SB();
      kloop<3, 256>(L, rws, W_X + TT * XB + lane * 16, tq, tq + 2 * U, a);
      row_stats(st, mean, var);
      STAMP(23);
      const float rs = __builtin_amdgcn_rsqf(var + 1e-5f);
      asm volatile("s_nop 7" : "+v"(a[0]), "+v"(a[1]));
      const int kvoff = W_KV + 1 * KV_L + (j * 64 + TT) * 4096 + lane * 16;
      v4u v0, v1;
      ln_pack(L, a[0], M_CSQ, h, -mean * rs, rs, 0.f, kf[0], kf[1]);
      ST16(kf[0], kvoff); ST16(kf[1], kvoff + 1024);
      ln_pack(L, a[1], M_CSQ + 128, h, -mean * rs, rs, 1.f, v0, v1);
      ST16(v0, kvoff + 2048); ST16(v1, kvoff + 3072);
      if (w < 3) put_k(L, 8 + w, lane, kf[0], kf[1]);
      put_v(L, 8 + w, lane, v0, v1);
      STAMP(19);
      const float sq = rs * (0.18898223650461362f * 1.4426950408889634f);
      ln_pack(L, a[2], M_CSQ + 256, h, -mean * sq, sq, 0.f, qf[0], qf[1]);
      }
#pragma unroll
      for (int r = 0; r < 16; ++r) {
        const bool past = (r & 3) + 8 * (r >> 2) + 4 * h > n;
        sc[0][r] = past ? 0.f : -1e30f;
        sc[8][r] = past ? -1e30f : 0.f;
      }
      asm volatile("s_nop 1" : "+v"(sc[0]), "+v"(sc[8]), "+v"(qf[0]), "+v"(qf[1]));
      mfma_vv(sc[8], kf[0], qf[0]); mfma_vv(sc[8], kf[1], qf[1]);
      VMWAIT(0);
      if (layer == 1 && lane < 2 && c + 1 + lane < 16) __hip_atomic_store(flag_line(ws, 1, E_KV + (lane ? 0 : 2) + (w >> 1), wg + 16 * (1 + lane)) + (w & 1), epoch, RLX);
      STAMP(2);
      if (layer == 0) {
        __builtin_amdgcn_s_barrier();
        if (w == 0 && (mlp || lane >= 48))
          __hip_atomic_store(lane < 32 ? flag_line(ws, 0, E_W2, (lane >> 1) * 16 + jb + 7 * (lane & 1)) + 2 * c + hf5 : flag_line(ws, 0, E_W1 + (lane >= 48), (lane & 15) * 16 + j) + c, epoch, RLX);
      }
#pragma unroll
      for (int q = 0; q < 4; ++q) {
        const int P = 7 * j + 2 * q + h;
        oaddr[q] = (q == 3 && h == 1) ? W_END : W_O + TT * XB + n * 16 + (P >> 2) * 1024 + (P & 1) * 512 + ((P >> 1) & 1) * 8;
        OPAQUE(oaddr[q]);
      }
      if (4 * c - 8 + 2 * w >= 0) poll(flag_line(ws, layer, E_KV + w, wg) + (lane & 1), epoch, dead);
      if (layer == 0) fw = __hip_atomic_load(fwp, RLX);
      STAMP(3);
    }
    FRESH();
    unsigned tp0 = tr_base(lane, 0, false, 0);
    {
      v4u hk[2][4];
      const int ho = 4 * c - 8 + 2 * w >= 0 ? W_KV + layer * KV_L + (j * 64 + 4 * c - 8 + 2 * w) * 4096 : W_END;
#pragma unroll
      for (int hh = 0; hh < 2; ++hh)
#pragma unroll
        for (int e = 0; e < 4; ++e) hk[hh][e] = __builtin_amdgcn_raw_buffer_load_b128(rws, lane * 16 + e * 1024, ho + hh * 4096, 16);
      float mx[4] = {-1e30f, -1e30f, -1e30f, -1e30f};
#define TMAX(T, a, b) do { _Pragma("unroll") for (int r_ = a; r_ < b; r_ += 2) mx[(r_ >> 1) & 3] = max3(mx[(r_ >> 1) & 3], T[r_], T[r_ + 1]); } while (0)
      MFMA_DONE(sc[8]);
      TMAX(sc[8], 0, 16);
      SB();
#pragma unroll
      for (int hh = 0; hh < 2; ++hh) {
        put_k(L, 2 * w + hh, lane, hk[hh][0], hk[hh][1]);
        put_v(L, 2 * w + hh, lane, hk[hh][2], hk[hh][3]);
      }
      __syncthreads();
      STAMP(4); STAMPC(27);
      if (layer == 0) {
        CHECK(fw, fwp);
        if (mlp) { dmaN<4>(rws, lane * 16, W_WOT + j * U + 7 * w * 1024, L_WO + 7 * w * 1024); dmaN<3>(rws, lane * 16, W_WOT + j * U + (7 * w + 4) * 1024, L_WO + (7 * w + 4) * 1024); }
      }
      const unsigned w1s = mlp ? W_W1T + (j * 112 + 21 * w) * 1024 : W_END, w1d = 21 * w * 1024, l16 = lane * 16;
      dmaN<3>(rws, l16, w1s, w1d);
      v4u kb[3][2];
      unsigned ka = L_K + w * 2048 + lane * 16;
      OPAQUE(ka);
#define KRD(i) do { kb[(i) % 3][0] = *(LAS v4u*)(L + ka + (i) * 2048); kb[(i) % 3][1] = *(LAS v4u*)(L + ka + (i) * 2048 + 1024); } while (0)
#define CLIP(i) do { if (__builtin_expect(TT - 8 + (i) < 0, 0)) { asm volatile("" ::: "memory"); _Pragma("unroll") for (int r_ = 0; r_ < 16; ++r_) sc[i][r_] = -1e30f; } } while (0)
      KRD(0); KRD(1); KRD(2);
      SB();
      mfma_vv(sc[0], kb[0][0], qf[0]); mfma_vv(sc[0], kb[0][1], qf[1]);
#pragma unroll
      for (int i = 1; i < 8; ++i) {
        mfma_v0(sc[i], kb[i % 3][0], qf[0]);
        asm volatile("s_nop 3" : "+v"(sc[i - 1]));
        CLIP(i - 1);
        TMAX(sc[i - 1], 0, 8);
        SB();
        mfma_vv(sc[i], kb[i % 3][1], qf[1]);
        if (i + 2 < 8) KRD(i + 2);
        TMAX(sc[i - 1], 8, 16);
        SB();
      }
      unsigned vb = L_V + w * 2048 + tp0;
      OPAQUE(vb);
      v4u vf[3];
      vf[0] = trf(L, vb, vb + 512);
      vf[1] = trf(L, vb + 1024, vb + 1536);
      MFMA_DONE(sc[7]);
      CLIP(7);
      TMAX(sc[7], 0, 16);
      float m = max3(max3(mx[0], mx[1], mx[2]), mx[3], mx[3]);
      m = max3(m, xh(m), m);
      const f32x2 nm = {-m, -m};
      f32x16 o;
#pragma unroll
      for (int st = 0; st < 18; ++st) {
        f32x2 e[4];
#pragma unroll
        for (int d = 0; d < 4; ++d) e[d] = f32x2{sc[st >> 1][8 * (st & 1) + 2 * d], sc[st >> 1][8 * (st & 1) + 2 * d + 1]} + nm;
        v4u pf;
        SB();
#pragma unroll
        for (int d = 0; d < 4; ++d) { e[d][0] = __builtin_amdgcn_exp2f(e[d][0]); e[d][1] = __builtin_amdgcn_exp2f(e[d][1]); }
        SB();
#pragma unroll
        for (int d = 0; d < 4; ++d) pf[d] = pk(e[d]);
        SB();
        if (st + 2 < 18) vf[(st + 2) % 3] = trf(L, vb + (st + 2) * 1024, vb + (st + 2) * 1024 + 512);
        else asm volatile("s_nop 1" : "+v"(pf));
        if (st % 2 == 0 && st < 12) dmaN<3>(rws, l16, w1s + (3 * (st / 2) + 3) * 1024, w1d + (3 * (st / 2) + 3) * 1024);
        if (st % 2 == 1 && st >= 7 && st < 17 && layer == 0 && mlp) W2PART(st / 2 - 3);
        SB();
        if (st == 0) mfma_v0(o, vf[0], pf); else mfma_vv(o, vf[st % 3], pf);
        SB();
      }
      MFMA_DONE(o);
      const float other = xh(o[12]);
      const float inv = __builtin_amdgcn_rcpf(h ? o[12] : other);
#pragma unroll
      for (int q = 0; q < 4; ++q) ST8((v2u{pk(f32x2{o[4 * q], o[4 * q + 1]} * inv), pk(f32x2{o[4 * q + 2], o[4 * q + 3]} * inv)}), oaddr[q]);
      STAMP(20); STAMPC(28);
      VMWAIT(0);
      __syncthreads();
      STAMP(5);
      if (w == 0 && lane < 14) __hip_atomic_store(flag_line(ws, layer, E_O, c * 16 + lane) + j, epoch, RLX);
    }
    if (mlp) {
      FRESH();
      dmaN<4>(rws, lane * 16, W_W1T + (j * 112 + 84 + 7 * w) * 1024, (84 + 7 * w) * 1024);
      dmaN<3>(rws, lane * 16, W_W1T + (j * 112 + 88 + 7 * w) * 1024, (88 + 7 * w) * 1024);
      v2u res[4];
#pragma unroll
      for (int q = 0; q < 4; ++q)
        res[q] = layer == 0 ? __builtin_amdgcn_raw_buffer_load_b64(rx, (32 * TT + n) * 896 + j * 64 + 8 * h + 16 * q, 0, 0) : LD8(W_X + TT * XB + 2 * j * 1024 + n * 16 + 8 * h + (q >> 1) * 1024 + (q & 1) * 512);
      if (w == 0) poll(flag_line(ws, layer, E_O, wg) + (lane & 15), epoch, dead);
      __syncthreads();
      STAMP(6);
      FRESH();
      {
        f32x16 a[1];
        kloop<1, 512>(L, rws, W_O + TT * XB + lane * 16, tp0 + L_WO, 0u, a);
        STAMP(7);
        MFMA_DONE(a[0]);
        unsigned o[8];
#pragma unroll
        for (int q = 0; q < 4; ++q) {
          o[2 * q] = pk(f32x2{a[0][4 * q], a[0][4 * q + 1]} + unpk(res[q].x));
          o[2 * q + 1] = pk(f32x2{a[0][4 * q + 2], a[0][4 * q + 3]} + unpk(res[q].y));
        }
        const int xo = W_X1 + TT * XB;
        ST16((v4u{o[0], o[1], o[2], o[3]}), xo + 2 * j * 1024 + lane * 16);
        ST16((v4u{o[4], o[5], o[6], o[7]}), xo + 2 * j * 1024 + 1024 + lane * 16);
        float s = 0.f, q2 = 0.f;
#pragma unroll
        for (int e = 0; e < 8; ++e) { s = dot2(o[e], ONES, s); q2 = dot2(o[e], o[e], q2); }
        s += xh(s); q2 += xh(q2);
        ST8((v2u{__builtin_bit_cast(unsigned, s), __builtin_bit_cast(unsigned, q2)}), h ? W_END : xo + X_ST + j * 256 + n * 8);
      }
      VMWAIT(0);
      __syncthreads();
      STAMP(8);
      if (w == 0 && lane < 14) __hip_atomic_store(flag_line(ws, layer, E_X1, c * 16 + lane) + j, epoch, RLX);
      if (layer == 0) {
        w2range<20, 30>(w2f, rws, W2V);
        f32x16 ca = zero16();
        colsum<0>(L, w * 1024 + lane * 16, 4, ca);
        colsum_put(L, ca, M_CS1 + w * 128, lane);
      }
      if (w == 0) poll(flag_line(ws, layer, E_X1, wg) + (lane < 14 ? lane : 0), epoch, dead);
      __syncthreads();
      STAMP(9); STAMPC(24);
      FRESH();
      {
        v2u st[7];
#pragma unroll
        for (int i = 0; i < 7; ++i) st[i] = LD8(W_X1 + TT * XB + X_ST + (7 * h + i) * 256 + n * 8);
        SB();
        f32x16 a[4];
        kloop<4, 0>(L, rws, W_X1 + TT * XB + lane * 16, lane * 16, 0u, a);
        STAMP(10); STAMPC(25);
        if (layer == 0) __builtin_amdgcn_s_barrier();
        f32x4 cs[4], bb1[4];
#pragma unroll
        for (int q = 0; q < 4; ++q) { cs[q] = *(LAS f32x4*)(L + M_CS1 + q * 32 + h * 16); bb1[q] = *(LAS f32x4*)(L + M_B1 + q * 32 + h * 16); }
        row_stats(st, mean, var);
        float rs = __builtin_amdgcn_rsqf(var + 1e-5f);
        rs = rs * __builtin_amdgcn_rsqf(var * rs * rs + 1e-5f);
        const float nmr = -mean * rs;
        asm volatile("s_nop 7" : "+v"(a[0]), "+v"(a[1]), "+v"(a[2]), "+v"(a[3]));
#pragma unroll
        for (int cc = 0; cc < 4; ++cc) {
          unsigned o[8];
          f32x2 v[8], z[8];
#pragma unroll
          for (int e = 0; e < 8; ++e) z[e] = f32x2{cs[e >> 1][2 * (e & 1)], cs[e >> 1][2 * (e & 1) + 1]} * nmr + f32x2{bb1[e >> 1][2 * (e & 1)], bb1[e >> 1][2 * (e & 1) + 1]};
          SB();
          if (cc < 3) {
#pragma unroll
            for (int q = 0; q < 4; ++q) { cs[q] = *(LAS f32x4*)(L + M_CS1 + (cc + 1) * 128 + q * 32 + h * 16); bb1[q] = *(LAS f32x4*)(L + M_B1 + (cc + 1) * 128 + q * 32 + h * 16); }
          }
          if (layer == 0) { if (cc == 0) w2range<30, 37>(w2f, rws, W2V); if (cc == 1) w2range<37, 44>(w2f, rws, W2V); if (cc == 2) w2range<44, 50>(w2f, rws, W2V); if (cc == 3) w2range<50, 56>(w2f, rws, W2V); }
#pragma unroll
          for (int e = 0; e < 8; ++e) v[e] = f32x2{a[cc][2 * e], a[cc][2 * e + 1]} * rs + z[e];
          SB();
#pragma unroll
          for (int e = 0; e < 8; ++e) z[e] = v[e] * v[e];
          SB();
#pragma unroll
          for (int e = 0; e < 8; ++e) z[e] = z[e] * -0.10294324f - 2.3022082f;
          SB();
#pragma unroll
          for (int e = 0; e < 8; ++e) z[e] *= v[e];
          SB();
#pragma unroll
          for (int e = 0; e < 8; ++e) { z[e][0] = __builtin_amdgcn_exp2f(z[e][0]); z[e][1] = __builtin_amdgcn_exp2f(z[e][1]); }
          SB();
#pragma unroll
          for (int e = 0; e < 8; ++e) z[e] += 1.0f;
          if (layer == 0) { if (cc < 3) dmaN<4>(rws, lane * 16, W_WQT + j * 3 * U + (14 * w + 4 * cc) * 1024, (14 * w + 4 * cc) * 1024); else dmaN<2>(rws, lane * 16, W_WQT + j * 3 * U + (14 * w + 12) * 1024, (14 * w + 12) * 1024); }
          SB();
#pragma unroll
          for (int e = 0; e < 8; ++e) { z[e][0] = __builtin_amdgcn_rcpf(z[e][0]); z[e][1] = __builtin_amdgcn_rcpf(z[e][1]); }
          SB();
#pragma unroll
          for (int e = 0; e < 8; ++e) v[e] *= z[e];
          SB();
#pragma unroll
          for (int e = 0; e < 8; ++e) o[e] = pk(v[e]);
          const int ho = W_H + TT * HB + (8 * j + 2 * cc) * 1024 + lane * 16;
          ST16((v4u{o[0], o[1], o[2], o[3]}), ho);
          ST16((v4u{o[4], o[5], o[6], o[7]}), ho + 1024);
          SB();
          if (cc == 2) {
            if (layer == 0) VMWAIT(12); else VMWAIT(2);
            __builtin_amdgcn_s_barrier();
            if (w == 0 && lane < 14) __hip_atomic_store(flag_line(ws, layer, E_H0, c * 16 + lane) + j, epoch, RLX);
          }
        }
      }
      STAMP(21); STAMPC(26);
      VMWAIT(0);
      __syncthreads();
      STAMP(11);
      if (w == 0 && lane < 14) __hip_atomic_store(flag_line(ws, layer, E_H1, c * 16 + lane) + j, epoch, RLX);
      FRESH();
      const int T5 = 4 * c + 2 * hf5, k5 = 4 * jb + w;
      v2u r5[2][2];
#pragma unroll
      for (int m = 0; m < 2; ++m)
#pragma unroll
        for (int e = 0; e < 2; ++e) r5[m][e] = LD8(W_X1 + (T5 + m) * XB + k5 * 1024 + e * 512 + n * 16 + 8 * h);
      const v4u bb2 = *(const v4u*)(b2 + k5 * 16 + 8 * h);
      if (w == 0) poll(flag_line(ws, layer, E_H0, wg) + (lane < 14 ? lane : 0), epoch, dead);
      __syncthreads();
      STAMP(12); STAMPC(29);
      {
        f32x16 acc[2][2];
        v4u hf[12];
        const unsigned hv = W_H + T5 * HB + w * 1024 + lane * 16;
        auto ldh = [&](int i) {
          const int so = (i < 14 ? 8 * i : 8 * (i - 14) + 4) * 1024;
          hf[2 * (i % 6)] = __builtin_amdgcn_raw_buffer_load_b128(rws, hv, so, 16);
          hf[2 * (i % 6) + 1] = __builtin_amdgcn_raw_buffer_load_b128(rws, hv, so + HB, 16);
        };
        GAS u64* fp1 = flag_line(ws, layer, E_H1, wg) + (lane < 14 ? lane : 0);
        u64 f1;
#pragma unroll
        for (int i = 0; i < 5; ++i) ldh(i);
        SB();
#pragma unroll
        for (int i = 0; i < 28; ++i) {
#pragma unroll
          for (int m = 0; m < 2; ++m) {
#pragma unroll
            for (int x = 0; x < 2; ++x) { if (i == 0) mfma_a0(acc[x][m], w2f[i][x], hf[m]); else mfma_av(acc[x][m], w2f[i][x], hf[2 * (i % 6) + m]); }
            SB();
            if (m == 0 && i == 4) f1 = __hip_atomic_load(fp1, RLX);
            if (m == 0 && i == 9) CHECK(f1, fp1);
            if (m == 0 && i + 5 < 28) ldh(i + 5);
            SB();
          }
        }
        STAMP(13); STAMPC(30);
        FRESH();
        asm volatile("s_nop 11" : "+v"(acc[0][0]), "+v"(acc[0][1]), "+v"(acc[1][0]), "+v"(acc[1][1]));
#pragma unroll
        for (int d = 0; d < 4; ++d)
#pragma unroll
          for (int x = 0; x < 2; ++x)
#pragma unroll
            for (int m = 0; m < 2; ++m)
              *(LAS f32x4*)(L + L_RED + w * 4096 + lane * 16 + d * 16384 + (x * 2 + m) * 1024) = f32x4{acc[x][m][4 * d], acc[x][m][4 * d + 1], acc[x][m][4 * d + 2], acc[x][m][4 * d + 3]};
        __syncthreads();
        f32x4 p[2][2];
#pragma unroll
        for (int s = 0; s < 4; ++s)
#pragma unroll
          for (int x = 0; x < 2; ++x)
#pragma unroll
            for (int m = 0; m < 2; ++m) { const f32x4 t = *(LAS f32x4*)(L + L_RED + w * 16384 + lane * 16 + s * 4096 + (x * 2 + m) * 1024); p[x][m] = s == 0 ? t : p[x][m] + t; }
#pragma unroll
        for (int m = 0; m < 2; ++m) {
          const unsigned rr[4] = {r5[m][0].x, r5[m][0].y, r5[m][1].x, r5[m][1].y};
          v4u o;
#pragma unroll
          for (int d = 0; d < 4; ++d) o[d] = pk(f32x2{p[d >> 1][m][2 * (d & 1)], p[d >> 1][m][2 * (d & 1) + 1]} + unpk(rr[d]) + unpk(bb2[d]));
          if (layer == 0) {
            ST16(o, W_X + (4 * c + 2 * hf5 + m) * XB + (4 * jb + w) * 1024 + lane * 16);
            float s = 0.f, q2 = 0.f;
#pragma unroll
            for (int e = 0; e < 4; ++e) { s = dot2(o[e], ONES, s); q2 = dot2(o[e], o[e], q2); }
            *(LAS f32x2*)(L + L_RST + ((m * 8 + w * 2 + h) * 32 + n) * 8) = f32x2{s, q2};
          } else {
            *(v4u*)(out + (32 * (4 * c + 2 * hf5 + m) + n) * 448 + (4 * jb + w) * 16 + 8 * h) = o;
          }
        }
      }
    } else if (layer == 1 && wg == 15 && w == 0) {
      bool ok = false;
      for (int spins = 0; spins < SPIN_CAP && !ok; ++spins) {
        bool a = true;
#pragma unroll
        for (int i = 0; i < 4; ++i) a &= __hip_atomic_load((GAS u64*)(ws + W_STARTED) + lane + 64 * i, RLX) == epoch;
        ok = __builtin_amdgcn_ballot_w64(!a) == 0;
      }
      if (lane == 0) __hip_atomic_store((GAS u64*)(ws + W_E), epoch, RLX);
    }
    if (layer == 0) {
      FRESH();
      __syncthreads();
      if (mlp && w < 2) {
        f32x2 t = {0.f, 0.f};
#pragma unroll
        for (int i = 0; i < 4; ++i) t += *(LAS f32x2*)(L + L_RST + ((h * 8 + 4 * w + i) * 32 + n) * 8);
        ST8(__builtin_bit_cast(v2u, t), W_X + (4 * c + 2 * hf5 + h) * XB + X_ST + (2 * jb + w) * 256 + n * 8);
      }
      VMWAIT(0);
      __syncthreads();
      STAMP(14);
      if (w == 0) {
        if (mlp && lane < 16) __hip_atomic_store(flag_line(ws, 1, E_X, c * 16 + lane) + j, epoch, RLX);
        poll(flag_line(ws, 1, E_X, wg) + (lane < 14 ? lane : 0), epoch, dead);
      } else {
        if (mlp) copy_in(rws, lane, W_WQT + j * 3 * U + (56 + 10 * (w - 1)) * 1024, (56 + 10 * (w - 1)) * 1024, w == 3 ? 8 : 10);
        else copy_in(rws, lane, W_WQT + (j * 3 + w - 1) * U, (w - 1) * U, 28);
        VMWAIT(0);
      }
      __syncthreads();
      STAMP(15);
    }
    asm volatile("" : "=v"(qf[0]), "=v"(qf[1]), "=v"(kf[0]), "=v"(kf[1]));
    STAMP(16);
  }
}
}

extern "C" void kernel_launch(void* const* d_in, const int* in_sizes, int n_in, void* d_out, int out_size, void* d_ws, size_t ws_size, hipStream_t stream) {
  (void)hipFuncSetAttribute((const void*)mk::fused, hipFuncAttributeMaxDynamicSharedMemorySize, mk::L_TOTAL);
  mk::fused<<<256, 256, mk::L_TOTAL, stream>>>((const unsigned short*)d_in[0], (const unsigned short*)d_in[1], (const unsigned short*)d_in[2],
      (const unsigned short*)d_in[4], (const unsigned short*)d_in[5], (const unsigned short*)d_in[6], (const unsigned short*)d_in[7],
      (unsigned short*)d_out, (unsigned char*)d_ws);
}
```

```cpp
#include <hip/hip_runtime.h>
#include <stdint.h>

namespace mk {
typedef unsigned v4u __attribute__((ext_vector_type(4)));
typedef unsigned v2u __attribute__((ext_vector_type(2)));
typedef float f32x4 __attribute__((ext_vector_type(4)));
typedef float f32x2 __attribute__((ext_vector_type(2)));
typedef float f32x16 __attribute__((ext_vector_type(16)));
typedef short s16x4 __attribute__((ext_vector_type(4)));
typedef __bf16 bf16x8 __attribute__((ext_vector_type(8)));
typedef __bf16 bf16x2 __attribute__((ext_vector_type(2)));
typedef unsigned long long u64;
typedef __amdgpu_buffer_rsrc_t rsrc_t;
#define LAS __attribute__((address_space(3)))
#define GAS __attribute__((address_space(1)))
#define RLX __ATOMIC_RELAXED, __HIP_MEMORY_SCOPE_AGENT
#define DI __device__ __forceinline__
#define SB() __builtin_amdgcn_sched_barrier(0)
#define OPAQUE(x) asm volatile("" : "+v"(x))

constexpr int SEQ = 2048, DM = 448, FF = 1792;
constexpr int U = 28672;
constexpr int L_K = 3 * U, L_V = L_K + 22 * 1024, L_WO = L_K + 46 * 1024, L_MISC = L_WO + U, L_TOTAL = 163840;
constexpr int L_RED = 2 * U, L_RST = L_RED + 65536;
constexpr int M_CSQ = L_MISC, M_CS1 = L_MISC + 384, M_B1 = L_MISC + 896, M_ST = L_MISC + 1408;
constexpr int L_XS = 2 * U;
constexpr int L_CSP = L_V;
static_assert(L_MISC + 2048 == L_TOTAL && L_XS + 4 * 24576 <= L_MISC, "LDS map");
constexpr int W_E = 0, W_STARTED = 4096, W_FLAG = 8192;
enum { E_KV = 0  , E_O = 4, E_X1 = 5, E_H0 = 6, E_H1 = 7, E_X = 8, E_W2 = 9  , E_WQ = 11, NEDGE = 12 };
constexpr int W_KV = 1 << 20, KV_L = 16 * 64 * 4096;
constexpr int XB = 32 * 1024, X_L = 64 * XB;
constexpr int X_ST = 28 * 1024;
constexpr int W_O = W_KV + 2 * KV_L, W_X1 = W_O + X_L, W_X = W_X1 + X_L, W_H = W_X + X_L;
constexpr int HB = 112 * 1024, H_L = 64 * HB;
constexpr int W_W2T = W_H + H_L;
constexpr int W_WQT = W_W2T + 7 * 112 * 2048;
constexpr int W_END = W_WQT + 16 * 3 * U;
constexpr int W_STAMP = W_END;
static_assert(W_FLAG + 2 * 256 * NEDGE * 128 <= W_KV, "flags");
constexpr int SPIN_CAP = 1 << 16;
constexpr int CODE_LINES = 236;
constexpr unsigned ONES = 0x3f803f80u;

DI f32x16 mfma(v4u a, v4u b, f32x16 c) {
  return __builtin_amdgcn_mfma_f32_32x32x16_bf16(__builtin_bit_cast(bf16x8, a), __builtin_bit_cast(bf16x8, b), c, 0, 0, 0);
}
DI void mfma_v0(f32x16& acc, v4u a, v4u b) { asm volatile("v_mfma_f32_32x32x16_bf16 %0, %1, %2, 0" : "=&v"(acc) : "v"(a), "v"(b)); }
DI void mfma_vv(f32x16& acc, v4u a, v4u b) { asm volatile("v_mfma_f32_32x32x16_bf16 %0, %1, %2, %0" : "+v"(acc) : "v"(a), "v"(b)); }
DI void mfma_a0(f32x16& acc, v4u a, v4u b) { asm volatile("v_mfma_f32_32x32x16_bf16 %0, %1, %2, 0" : "=&v"(acc) : "a"(a), "v"(b)); }
DI void mfma_av(f32x16& acc, v4u a, v4u b) { asm volatile("v_mfma_f32_32x32x16_bf16 %0, %1, %2, %0" : "+v"(acc) : "a"(a), "v"(b)); }
#define MFMA_DONE(x) asm volatile("s_nop 11" : "+v"(x))
DI v2u trd(LAS unsigned char* L, unsigned a) {
  return __builtin_bit_cast(v2u, __builtin_amdgcn_ds_read_tr16_b64_v4i16((LAS s16x4*)(L + a)));
}
DI v4u trf(LAS unsigned char* L, unsigned a0, unsigned a1) { const v2u p = trd(L, a0), q = trd(L, a1); return v4u{p.x, p.y, q.x, q.y}; }
DI unsigned pk(float a, float b) { f32x2 f = {a, b}; return __builtin_bit_cast(unsigned, __builtin_convertvector(f, bf16x2)); }
DI unsigned pk(f32x2 f) { return __builtin_bit_cast(unsigned, __builtin_convertvector(f, bf16x2)); }
DI float blo(unsigned u) { return __builtin_bit_cast(float, u << 16); }
DI float bhi(unsigned u) { return __builtin_bit_cast(float, u & 0xffff0000u); }
DI f32x2 unpk(unsigned u) { return f32x2{blo(u), bhi(u)}; }
DI int rfl(int x) { return __builtin_amdgcn_readfirstlane(x); }
DI float dot2(unsigned a, unsigned b, float c) { return __builtin_amdgcn_fdot2_f32_bf16(__builtin_bit_cast(bf16x2, a), __builtin_bit_cast(bf16x2, b), c, false); }
DI float max3(float a, float b, float c) { float r; asm("v_max3_f32 %0, %1, %2, %3" : "=v"(r) : "v"(a), "v"(b), "v"(c)); return r; }
DI float xh(float v) {
  const unsigned u = __builtin_bit_cast(unsigned, v);
  const auto r = __builtin_amdgcn_permlane32_swap(u, u, false, false);
  return __builtin_bit_cast(float, (threadIdx.x & 32) ? r[0] : r[1]);
}
DI rsrc_t mkrs(const void* p, int bytes) { return __builtin_amdgcn_make_buffer_rsrc((void*)p, 0, bytes, 0x00020000); }
DI void dma16(rsrc_t rs, unsigned voff, unsigned soff, unsigned ldst) {
  asm volatile("s_mov_b32 m0, %0\n\ts_nop 0\n\tbuffer_load_dwordx4 %1, %2, %3 offen lds" :: "s"(ldst), "v"(voff), "s"(rs), "s"(soff) : "memory");
}
template <int N> DI void dmaN(rsrc_t rs, unsigned voff, unsigned soff, unsigned ldst) {
  if constexpr (N == 4)
    asm volatile("s_mov_b32 m0, %0\n\ts_nop 0\n\tbuffer_load_dwordx4 %1, %2, %3 offen sc1 lds\n\tbuffer_load_dwordx4 %1, %2, %3 offen offset:1024 sc1 lds\n\t"
                 "buffer_load_dwordx4 %1, %2, %3 offen offset:2048 sc1 lds\n\tbuffer_load_dwordx4 %1, %2, %3 offen offset:3072 sc1 lds" :: "s"(ldst), "v"(voff), "s"(rs), "s"(soff) : "memory");
  else
    asm volatile("s_mov_b32 m0, %0\n\ts_nop 0\n\tbuffer_load_dwordx4 %1, %2, %3 offen sc1 lds\n\tbuffer_load_dwordx4 %1, %2, %3 offen offset:1024 sc1 lds\n\t"
                 "buffer_load_dwordx4 %1, %2, %3 offen offset:2048 sc1 lds" :: "s"(ldst), "v"(voff), "s"(rs), "s"(soff) : "memory");
}
#define VMWAIT(n) asm volatile("s_waitcnt vmcnt(" #n ")" ::: "memory")
#define ST16(v, off) __builtin_amdgcn_raw_buffer_store_b128(v, rws, off, 0, 16)
#define ST8(v, off) __builtin_amdgcn_raw_buffer_store_b64(v, rws, off, 0, 16)
#define LD8(off) __builtin_amdgcn_raw_buffer_load_b64(rws, off, 0, 16)
DI unsigned tr_base(int lane, int tp, bool nat, int par) {
  const int h = lane >> 5, cb = (lane >> 4) & 1, q = (lane >> 2) & 3, p = lane & 3;
  const int row = nat ? 8 * h + 4 * tp + q : 8 * tp + 4 * h + q;
  return row * 64 + ((4 * cb + p + par) & 7) * 8;
}
DI void xwait6(v4u& x) { asm volatile("s_waitcnt vmcnt(6)" : "+v"(x) :: "memory"); }
template <int I> DI void xload(v4u& x, rsrc_t rs, unsigned voff, unsigned s0, unsigned s1) {
  asm volatile("buffer_load_dwordx4 %0, %1, %2, %3 offen offset:%4 sc1" : "=v"(x) : "v"(voff), "s"(rs), "s"(I < 4 ? s0 : s1), "i"((I & 3) * 1024) : "memory");
}
template <int I> DI void w2load(v4u (&w2f)[28][2], rsrc_t rs, unsigned voff) {
  asm volatile("buffer_load_dwordx4 %0, %1, %2, %3 offen offset:%4 sc1" : "=a"(w2f[I >> 1][I & 1]) : "v"(voff), "s"(rs),
               "s"(((I >> 1) < 14 ? 8 * (I >> 1) : 8 * ((I >> 1) - 14) + 4) * 2048), "i"((I & 1) * 1024) : "memory");
}
template <int LO, int HI> DI void w2range(v4u (&w2f)[28][2], rsrc_t rs, unsigned voff) {
  if constexpr (LO < HI) { w2load<LO>(w2f, rs, voff); w2range<LO + 1, HI>(w2f, rs, voff); }
}
DI unsigned tr_w1(int lane, int cc) {
  const int h = lane >> 5, cb = (lane >> 4) & 1, q = (lane >> 2) & 3, p = lane & 3;
  return (4 * h + q) * 256 + ((cc ^ q) * 64) + (4 * cb + p) * 8;
}
template <int NC, int KS, int TR1, int S> DI void kstep(LAS unsigned char* L, v4u (&xr)[7], rsrc_t rs, unsigned voff, unsigned s0, unsigned s1, const unsigned (&b)[NC],
                                                        v4u (&wb)[NC == 1 ? 7 : NC], f32x16 (&acc)[NC]) {
  if constexpr (S < 7) {
    xwait6(xr[S]);
    SB();
#pragma unroll
    for (int q = 0; q < NC; ++q) {
      mfma_vv(acc[q], wb[NC == 1 ? S : q], xr[S]);
      SB();
      constexpr int o = (S + (NC == 1 ? 7 : 1)) * KS;
      wb[NC == 1 ? S : q] = trf(L, b[q] + o, b[q] + o + TR1);
      if (q == NC - 1) xload<S>(xr[S], rs, voff, s0, s1);
      SB();
    }
    kstep<NC, KS, TR1, S + 1>(L, xr, rs, voff, s0, s1, b, wb, acc);
  }
}
template <int I> DI void xfirst(v4u (&xr)[7], rsrc_t rs, unsigned voff) { if constexpr (I < 7) { xload<I>(xr[I], rs, voff, 0u, 4096u); xfirst<I + 1>(xr, rs, voff); } }
template <int NC, int KS, int TR1> DI void kloop(LAS unsigned char* L, rsrc_t rs, unsigned voff, unsigned (&b)[NC], f32x16 (&acc)[NC]) {
  v4u xr[7], wb[NC == 1 ? 7 : NC];
  xfirst<0>(xr, rs, voff);
#pragma unroll
  for (int q = 0; q < NC; ++q) OPAQUE(b[q]);
#pragma unroll
  for (int i = 0; i < (NC == 1 ? 7 : NC); ++i) { const unsigned a = NC == 1 ? b[0] + i * KS : b[i]; wb[i] = trf(L, a, a + TR1); }
#pragma unroll
  for (int q = 0; q < NC; ++q) {
#pragma unroll
    for (int r = 0; r < 16; ++r) acc[q][r] = 0.f;
    asm volatile("" : "+v"(acc[q]));
  }
#pragma unroll 1
  for (int it = 1; it <= 4; ++it) {
    const unsigned s0 = it < 4 ? it * 7168u : (unsigned)W_END;
    kstep<NC, KS, TR1, 0>(L, xr, rs, voff, s0, s0 + 4096u, b, wb, acc);
#pragma unroll
    for (int q = 0; q < NC; ++q) b[q] += 7 * KS;
  }
  asm volatile("s_waitcnt vmcnt(0)" : "+v"(xr[0]), "+v"(xr[1]), "+v"(xr[2]), "+v"(xr[3]), "+v"(xr[4]), "+v"(xr[5]), "+v"(xr[6]) :: "memory");
}
template <int NC, class F> DI void kregs(LAS unsigned char* L, const v4u (&xf)[28], unsigned b, f32x16* acc, F bg) {
  constexpr int PD = 4 / NC;
  v4u wb[PD][NC];
  OPAQUE(b);
#pragma unroll
  for (int s = 0; s < PD; ++s)
#pragma unroll
    for (int q = 0; q < NC; ++q) wb[s][q] = trf(L, b + q * U + s * 1024, b + q * U + s * 1024 + 256);
#pragma unroll
  for (int s = 0; s < 28; ++s)
#pragma unroll
    for (int q = 0; q < NC; ++q) {
      SB();
      if (s == 0) mfma_v0(acc[q], wb[0][q], xf[0]); else mfma_vv(acc[q], wb[s % PD][q], xf[s]);
      SB();
      if (s + PD < 28) wb[s % PD][q] = trf(L, b + q * U + (s + PD) * 1024, b + q * U + (s + PD) * 1024 + 256);
      if (q == 0) bg(s);
    }
}
template <int KS, int TR1> DI void colsum(LAS unsigned char* L, unsigned b, int rounds, f32x16& a) {
  const v4u ones = {ONES, ONES, ONES, ONES};
#pragma unroll 1
  for (int s = 0; s < rounds; ++s) {
    OPAQUE(b);
    v4u f[7];
#pragma unroll
    for (int i = 0; i < 7; ++i) f[i] = trf(L, b + i * KS, b + i * KS + TR1);
    SB();
#pragma unroll
    for (int i = 0; i < 7; ++i) a = mfma(f[i], ones, a);
    b += 7 * KS;
  }
}
DI void colsum_put(LAS unsigned char* L, const f32x16& a, unsigned dst, int lane) {
  if ((lane & 31) == 0) {
#pragma unroll
    for (int q = 0; q < 4; ++q) *(LAS f32x4*)(L + dst + q * 32 + (lane >> 5) * 16) = f32x4{a[4 * q], a[4 * q + 1], a[4 * q + 2], a[4 * q + 3]};
  }
}
DI f32x16 zero16() { f32x16 a;
#pragma unroll
  for (int r = 0; r < 16; ++r) a[r] = 0.f;
  return a; }
DI GAS u64* flag_line(unsigned char* ws, int layer, int edge, int wg) { return (GAS u64*)(ws + W_FLAG + ((layer * 256 + wg) * NEDGE + edge) * 128); }
DI void poll(GAS u64* p, u64 epoch, bool& dead) {
  if (dead) return;
  for (int spins = 0;; ++spins) {
    const u64 f = __hip_atomic_load(p, RLX);
    if (__builtin_amdgcn_ballot_w64(f != epoch) == 0) break;
    if (spins > SPIN_CAP) { dead = true; break; }
  }
  __builtin_amdgcn_fence(__ATOMIC_ACQUIRE, "wavefront");
}
#define CHECK(f, p) do { if (__builtin_amdgcn_ballot_w64((f) != epoch) != 0) poll(p, epoch, dead); __builtin_amdgcn_fence(__ATOMIC_ACQUIRE, "wavefront"); } while (0)
#define STAMP(i)
#define STAMPC(i)
DI void put_k(LAS unsigned char* L, int lt, int lane, v4u a, v4u b) {
  *(LAS v4u*)(L + L_K + lt * 2048 + lane * 16) = a;
  *(LAS v4u*)(L + L_K + lt * 2048 + 1024 + lane * 16) = b;
}
DI void put_v(LAS unsigned char* L, int lt, int lane, v4u a, v4u b) {
  const unsigned o = L_V + lt * 2048 + (lane & 31) * 64 + (lane >> 5) * 8;
  *(LAS v2u*)(L + o) = v2u{a.x, a.y};
  *(LAS v2u*)(L + o + 16) = v2u{a.z, a.w};
  *(LAS v2u*)(L + o + 32) = v2u{b.x, b.y};
  *(LAS v2u*)(L + o + 48) = v2u{b.z, b.w};
}
DI void ln_pack(LAS unsigned char* L, const f32x16& a, unsigned cs, int h, float nmsc, float sc, float pad0, v4u& o0, v4u& o1) {
  unsigned o[8];
  f32x4 cq[4];
  f32x2 t[8];
#pragma unroll
  for (int q = 0; q < 4; ++q) cq[q] = *(LAS f32x4*)(L + cs + q * 32 + h * 16);
#pragma unroll
  for (int e = 0; e < 8; ++e) t[e] = f32x2{a[2 * e], a[2 * e + 1]} * sc;
  SB();
#pragma unroll
  for (int e = 0; e < 8; ++e) t[e] += f32x2{cq[e >> 1][2 * (e & 1)], cq[e >> 1][2 * (e & 1) + 1]} * nmsc;
  SB();
#pragma unroll
  for (int e = 0; e < 8; ++e) o[e] = pk(t[e]);
  if (h == 1) { o[6] = pk(pad0, 0.f); o[7] = 0u; }
  o0 = v4u{o[0], o[1], o[2], o[3]}; o1 = v4u{o[4], o[5], o[6], o[7]};
}
DI void row_stats(const v2u (&st)[7], float& mean, float& var) {
  f32x2 t = __builtin_bit_cast(f32x2, st[0]);
#pragma unroll
  for (int i = 1; i < 7; ++i) t += __builtin_bit_cast(f32x2, st[i]);
  mean = (t[0] + xh(t[0])) * (1.0f / DM);
  var = fmaxf((t[1] + xh(t[1])) * (1.0f / DM) - mean * mean, 0.f);
}

__global__ void __launch_bounds__(256, 1) fused(const unsigned short* __restrict__ xin, const unsigned short* __restrict__ wqkv, const unsigned short* __restrict__ wo,
    const unsigned short* __restrict__ w1, const unsigned short* __restrict__ b1, const unsigned short* __restrict__ w2, const unsigned short* __restrict__ b2,
    unsigned short* __restrict__ out, unsigned char* ws) {
  extern __shared__ __attribute__((aligned(16))) unsigned char smem[];
  LAS unsigned char* L = (LAS unsigned char*)smem;
  int lane = threadIdx.x & 63, w = rfl(threadIdx.x >> 6), n = lane & 31, h = lane >> 5;
  int c = 2 * (blockIdx.x & 7) + (blockIdx.x >> 7), j = (blockIdx.x >> 3) & 15;
  const bool mlp = j < 14;
  const int c0_ = c, j0_ = j, w0_ = w, lane0_ = lane;
#define wg (c * 16 + j)
#define TT (4 * c + w)
#define hf5 (j >= 7 ? 1 : 0)
#define jb (j >= 7 ? j - 7 : j)
#define W2V (W_W2T + (jb * 112 + w) * 2048 + lane * 16)
#define W1PIECE(i) dma16(rw1, (lane >> 4) * 3584 + (((lane & 15) ^ ((lane >> 4) << 2)) * 16), (i) * 4 * 3584 + j * 256, (i) * 1024)
#define WQ7(i) do { dmaN<4>(rws, lane * 16, W_WQT + j * 3 * U + (i) * 1024, (i) * 1024); dmaN<3>(rws, lane * 16, W_WQT + j * 3 * U + ((i) + 4) * 1024, ((i) + 4) * 1024); } while (0)
#define FRESH() do { c = c0_; j = j0_; w = w0_; lane = lane0_; asm volatile("" : "+s"(c), "+s"(j), "+s"(w)); asm volatile("" : "+v"(lane)); n = lane & 31; h = lane >> 5; } while (0)
  const rsrc_t rx = mkrs(xin, SEQ * DM * 2), rq = mkrs(wqkv, DM * 3 * DM * 2), rw1 = mkrs(w1, mlp ? DM * FF * 2 : 0);
  STAMP(0);
  unsigned ctouch;
  {
    const unsigned char* pc = (const unsigned char*)__builtin_amdgcn_s_getpc();
    const int part = (j >> 2) + 4 * (c & 1);
    asm volatile("global_load_dword %0, %1, off" : "=v"(ctouch) : "v"(pc + ((w == (j & 3) && part * 64 + lane < CODE_LINES) ? part * 8192 + lane * 128 : 0)) : "memory");
  }
  v4u xf[28];
  {
    const unsigned vo = (32 * TT + (lane >> 3)) * 896;
    int cb = j >= 12 ? j - 12 : (j >= 6 ? j - 6 : j);
#pragma unroll 1
    for (int t = 0; t < 6; ++t) {
#pragma unroll
      for (int r = 0; r < 4; ++r) dma16(rx, vo + (((lane & 7) ^ (((lane >> 4) + 4 * r) & 7)) * 16), cb * 128 + r * 8 * 896, L_XS + w * 24576 + cb * 4096 + r * 1024);
      cb = cb == 5 ? 0 : cb + 1;
    }
#pragma unroll
    for (int i = 24; i < 28; ++i) asm volatile("buffer_load_dwordx4 %0, %1, %2, 0 offen offset:%3" : "=v"(xf[i]) : "v"((32 * TT + n) * 896 + 16 * h), "s"(rx), "i"(32 * i) : "memory");
    const unsigned vq = (lane >> 2) * 2688 + (lane & 3) * 16;
    const int i0 = 7 * ((w + 2 * (c & 1)) & 3);
#pragma unroll
    for (int i = 0; i < 14; ++i) dma16(rq, vq, (i0 + (i >> 1)) * 16 * 2688 + 896 + (i & 1) * 896 + ((56 * j) & ~15), (i & 1) * U + (i0 + (i >> 1)) * 1024);
  }
  unsigned touch;
  {
    const unsigned char* p = ws + 4096 + (size_t)(lane & 15) * (2u << 20);
    if (lane >= 16) p = (const unsigned char*)w1 + (7 * c >> 2) * 16 * 3584;
    if (lane == 17) p = (const unsigned char*)w2 + 7 * c * 16 * 896;
    if (lane == 18) p = (const unsigned char*)wo + c * 16 * 896;
    if (lane == 19) p = (const unsigned char*)b1;
    if (lane == 20) p = (const unsigned char*)b2;
    if (lane == 21) p = (const unsigned char*)out + c * 128 * 896;
    asm volatile("global_load_dword %0, %1, off" : "=v"(touch) : "v"(p) : "memory");
  }
  const rsrc_t rws = mkrs(ws, W_END);
  int layer = 0;
  STAMP(31 - 0 * layer);
  const u64 epoch = *(const u64*)(ws + W_E) + 1;
  bool dead = false;
  v4u qf[2], kf[2];
  {
      float mean, var;
      f32x16 a[3];
      v2u wo1;
      const unsigned tq = tr_base(lane, 0, true, j & 1);
        asm volatile("s_waitcnt vmcnt(15)" : "+v"(xf[24]), "+v"(xf[25]), "+v"(xf[26]), "+v"(xf[27]) :: "memory");
        STAMP(22);
        const unsigned xb = L_XS + w * 24576 + n * 128;
#pragma unroll
        for (int i = 0; i < 24; ++i) xf[i] = *(LAS v4u*)(L + xb + (((2 * (i & 3) + h) ^ ((n >> 1) & 7)) * 16) + (i >> 2) * 4096);
        float s[2] = {0.f, 0.f}, q[2] = {0.f, 0.f};
#pragma unroll
        for (int i = 0; i < 28; ++i)
#pragma unroll
          for (int e = 0; e < 4; ++e) { s[e & 1] = dot2(xf[(i + 24) % 28][e], ONES, s[e & 1]); q[e & 1] = dot2(xf[(i + 24) % 28][e], xf[(i + 24) % 28][e], q[e & 1]); }
        mean = s[0] + s[1]; var = q[0] + q[1];
        mean = (mean + xh(mean)) * (1.0f / DM);
        var = fmaxf((var + xh(var)) * (1.0f / DM) - mean * mean, 0.f);
        STAMP(17);
        asm volatile("s_waitcnt vmcnt(0)" : "+v"(touch), "+v"(ctouch) :: "memory");
        __syncthreads();
        STAMP(18);
        const unsigned vq = (lane >> 2) * 2688 + (lane & 3) * 16;
        const int i0 = 7 * ((w + 2 * (c & 1)) & 3);
        const rsrc_t rw2 = mkrs(w2, mlp ? FF * DM * 2 : 0);
        const int so = ((7 * c + 4 * hf5 + w) * 16) * 896 + jb * 128;
        auto bg = [&](int s) {
          if (s % 4 == 0) dma16(rq, vq, (i0 + s / 4) * 16 * 2688 + ((56 * j) & ~15), 2 * U + (i0 + s / 4) * 1024);
          if (s == 2 || s == 6) dma16(rw2, (lane >> 3) * 896 + (lane & 7) * 16, so + (s / 4) * 8 * 896, L_K + w * 2048 + (s / 4) * 1024);
          if (s == 10) wo1.x = __builtin_amdgcn_raw_buffer_load_b32(mkrs(b1, mlp ? FF * 2 : 0), 256 * j + 4 * lane, 0, 0);
          if (s == 14) wo1.y = __builtin_amdgcn_raw_buffer_load_b32(rws, W_FLAG + (((lane >> 4) & 1) * 256 + wg) * NEDGE * 128 + (lane & 15) * 128, 0, 16);
        };
        if (threadIdx.x == 0) __hip_atomic_store((GAS u64*)(ws + W_STARTED) + wg, epoch, RLX);
        SB();
        STAMP(1);
#pragma unroll 1
        for (int im = 0; im < 2; ++im) { f32x16 ca = zero16(); colsum<1024, 256>(L, tq + im * U + 7 * w * 1024, 1, ca); colsum_put(L, ca, L_CSP + (im * 4 + w) * 128, lane); }
        STAMP(14);
        kregs<2>(L, xf, tq, a, bg);
        STAMP(15);
        __syncthreads();
        if (lane < 16) {
          const unsigned o = (lane >> 3) * 512 + (lane & 7) * 16;
          *(LAS f32x4*)(L + M_CSQ + lane * 16) = (*(LAS f32x4*)(L + L_CSP + o) + *(LAS f32x4*)(L + L_CSP + o + 128)) + (*(LAS f32x4*)(L + L_CSP + o + 256) + *(LAS f32x4*)(L + L_CSP + o + 384));
        }
      STAMP(23);
      const float rs = __builtin_amdgcn_rsqf(var + 1e-5f);
      asm volatile("s_nop 7" : "+v"(a[0]), "+v"(a[1]));
      const int kvoff = W_KV + 0 * KV_L + (j * 64 + TT) * 4096 + lane * 16;
      v4u v0, v1;
      ln_pack(L, a[0], M_CSQ, h, -mean * rs, rs, 0.f, kf[0], kf[1]);
      ST16(kf[0], kvoff); ST16(kf[1], kvoff + 1024);
      ln_pack(L, a[1], M_CSQ + 128, h, -mean * rs, rs, 1.f, v0, v1);
      ST16(v0, kvoff + 2048); ST16(v1, kvoff + 3072);
      if (w < 3) put_k(L, 8 + w, lane, kf[0], kf[1]);
      put_v(L, 8 + w, lane, v0, v1);
      STAMP(19);
      {
        VMWAIT(4);
        asm volatile("" :: "v"(wo1.y));
        __syncthreads();
        STAMP(6);
        { f32x16 ca = zero16(); colsum<1024, 256>(L, tq + 2 * U + 7 * w * 1024, 1, ca); colsum_put(L, ca, L_CSP + (8 + w) * 128, lane); }
        VMWAIT(0);
        if (lane < 2 && c + 1 + lane < 16) __hip_atomic_store(flag_line(ws, 0, E_KV + (lane ? 0 : 2) + (w >> 1), wg + 16 * (1 + lane)) + (w & 1), epoch, RLX);
        STAMP(7);
        {
          const unsigned ta = L_K + w * 2048 + (4 * h + ((lane >> 2) & 3)) * 128 + (8 * ((lane >> 4) & 1) + 4 * ((lane >> 1) & 1) + 2 * (lane & 1)) * 8;
          const int o2 = (mlp && w + hf5 < 4) ? W_W2T + (jb * 112 + 7 * c + 4 * hf5 + w) * 2048 + lane * 16 : W_END;
          ST16(trf(L, ta, ta + 1024), o2);
          ST16(trf(L, ta + 8, ta + 1032), o2 + 1024);
          if (w < 3) {
            ST16(*(LAS v4u*)(L + w * U + c * 1024 + lane * 16), W_WQT + (j * 3 + w) * U + c * 1024 + lane * 16);
            if (c < 12) ST16(*(LAS v4u*)(L + w * U + (c + 16) * 1024 + lane * 16), W_WQT + (j * 3 + w) * U + (c + 16) * 1024 + lane * 16);
          } else {
            *(LAS f32x2*)(L + M_B1 + lane * 8) = unpk(wo1.x);
          }
        }
        kregs<1>(L, xf, tq + 2 * U, a + 2, [](int) {});
        __syncthreads();
        if (lane < 8) {
          const unsigned o = 1024 + lane * 16;
          *(LAS f32x4*)(L + M_CSQ + 256 + lane * 16) = (*(LAS f32x4*)(L + L_CSP + o) + *(LAS f32x4*)(L + L_CSP + o + 128)) + (*(LAS f32x4*)(L + L_CSP + o + 256) + *(LAS f32x4*)(L + L_CSP + o + 384));
        }
        MFMA_DONE(a[2]);
        STAMP(9);
      }
      const float sq = rs * (0.18898223650461362f * 1.4426950408889634f);
      ln_pack(L, a[2], M_CSQ + 256, h, -mean * sq, sq, 0.f, qf[0], qf[1]);
  }
  v4u w2f[28][2];
#pragma unroll
  for (int i = 0; i < 28; ++i) { asm volatile("" : "=a"(w2f[i][0])); asm volatile("" : "=a"(w2f[i][1])); }

  for (; layer < 2; ++layer) {
    FRESH();
    float mean, var;
    f32x16 sc[9];
    int oaddr[4];
    {
      if (layer == 1) {
      STAMP(1);
      f32x16 a[3];
      const unsigned tq = tr_base(lane, 0, true, j & 1);
      v2u st[7];
#pragma unroll
      for (int i = 0; i < 7; ++i) st[i] = LD8(W_X + TT * XB + X_ST + (7 * h + i) * 256 + n * 8);
      SB();
      unsigned bq[3] = {tq, tq + U, tq + 2 * U};
      kloop<3, 1024, 256>(L, rws, W_X + TT * XB + lane * 16, bq, a);
      row_stats(st, mean, var);
      STAMP(23);
      const float rs = __builtin_amdgcn_rsqf(var + 1e-5f);
      asm volatile("s_nop 7" : "+v"(a[0]), "+v"(a[1]));
      const int kvoff = W_KV + 1 * KV_L + (j * 64 + TT) * 4096 + lane * 16;
      v4u v0, v1;
      ln_pack(L, a[0], M_CSQ, h, -mean * rs, rs, 0.f, kf[0], kf[1]);
      ST16(kf[0], kvoff); ST16(kf[1], kvoff + 1024);
      ln_pack(L, a[1], M_CSQ + 128, h, -mean * rs, rs, 1.f, v0, v1);
      ST16(v0, kvoff + 2048); ST16(v1, kvoff + 3072);
      if (w < 3) put_k(L, 8 + w, lane, kf[0], kf[1]);
      put_v(L, 8 + w, lane, v0, v1);
      STAMP(19);
      const float sq = rs * (0.18898223650461362f * 1.4426950408889634f);
      ln_pack(L, a[2], M_CSQ + 256, h, -mean * sq, sq, 0.f, qf[0], qf[1]);
      }
#pragma unroll
      for (int r = 0; r < 16; ++r) {
        const bool past = (r & 3) + 8 * (r >> 2) + 4 * h > n;
        sc[0][r] = past ? 0.f : -1e30f;
        sc[8][r] = past ? -1e30f : 0.f;
      }
      asm volatile("s_nop 1" : "+v"(sc[0]), "+v"(sc[8]), "+v"(qf[0]), "+v"(qf[1]));
      mfma_vv(sc[8], kf[0], qf[0]); mfma_vv(sc[8], kf[1], qf[1]);
      if (layer == 1) {
        VMWAIT(0);
        if (lane < 2 && c + 1 + lane < 16) __hip_atomic_store(flag_line(ws, 1, E_KV + (lane ? 0 : 2) + (w >> 1), wg + 16 * (1 + lane)) + (w & 1), epoch, RLX);
      } else {
        __builtin_amdgcn_s_barrier();
      }
      STAMP(2);
#pragma unroll
      for (int q = 0; q < 4; ++q) {
        const int P = 7 * j + 2 * q + h;
        oaddr[q] = (q == 3 && h == 1) ? W_END : W_O + TT * XB + n * 16 + (P >> 2) * 1024 + (P & 1) * 512 + ((P >> 1) & 1) * 8;
        OPAQUE(oaddr[q]);
      }
      if (4 * c - 8 + 2 * w >= 0) poll(flag_line(ws, layer, E_KV + w, wg) + (lane & 1), epoch, dead);
      STAMP(3);
    }
    FRESH();
    unsigned tp0 = tr_base(lane, 0, false, 0);
    {
      v4u hk[2][4];
      const int ho = 4 * c - 8 + 2 * w >= 0 ? W_KV + layer * KV_L + (j * 64 + 4 * c - 8 + 2 * w) * 4096 : W_END;
#pragma unroll
      for (int hh = 0; hh < 2; ++hh)
#pragma unroll
        for (int e = 0; e < 4; ++e) hk[hh][e] = __builtin_amdgcn_raw_buffer_load_b128(rws, lane * 16 + e * 1024, ho + hh * 4096, 16);
      float mx[4] = {-1e30f, -1e30f, -1e30f, -1e30f};
#define TMAX(T, a, b) do { _Pragma("unroll") for (int r_ = a; r_ < b; r_ += 2) mx[(r_ >> 1) & 3] = max3(mx[(r_ >> 1) & 3], T[r_], T[r_ + 1]); } while (0)
      MFMA_DONE(sc[8]);
      TMAX(sc[8], 0, 16);
      SB();
#pragma unroll
      for (int hh = 0; hh < 2; ++hh) {
        put_k(L, 2 * w + hh, lane, hk[hh][0], hk[hh][1]);
        put_v(L, 2 * w + hh, lane, hk[hh][2], hk[hh][3]);
      }
      __syncthreads();
      STAMP(4); STAMPC(27);
      if (layer == 0) {
        if (w == 0 && lane < 48 && (mlp || lane >= 32))
          __hip_atomic_store(lane < 32 ? flag_line(ws, 0, E_W2, (lane >> 1) * 16 + jb + 7 * (lane & 1)) + 2 * c + hf5 : flag_line(ws, 0, E_WQ, (lane & 15) * 16 + j) + c, epoch, RLX);
        const rsrc_t ro = mkrs(wo, mlp ? DM * DM * 2 : 0);
#pragma unroll 1
        for (int i = 7 * w; i < 7 * w + 4; ++i) dma16(ro, (lane >> 2) * 896 + (lane & 3) * 16, i * 16 * 896 + j * 64, L_WO + i * 1024);
      }
      W1PIECE(21 * w); W1PIECE(21 * w + 1); W1PIECE(21 * w + 2);
      v4u kb[3][2];
      unsigned ka = L_K + w * 2048 + lane * 16;
      OPAQUE(ka);
#define KRD(i) do { kb[(i) % 3][0] = *(LAS v4u*)(L + ka + (i) * 2048); kb[(i) % 3][1] = *(LAS v4u*)(L + ka + (i) * 2048 + 1024); } while (0)
#define CLIP(i) do { if (__builtin_expect(TT - 8 + (i) < 0, 0)) { asm volatile("" ::: "memory"); _Pragma("unroll") for (int r_ = 0; r_ < 16; ++r_) sc[i][r_] = -1e30f; } } while (0)
      KRD(0); KRD(1); KRD(2);
      SB();
      mfma_vv(sc[0], kb[0][0], qf[0]); mfma_vv(sc[0], kb[0][1], qf[1]);
#pragma unroll
      for (int i = 1; i < 8; ++i) {
        mfma_v0(sc[i], kb[i % 3][0], qf[0]);
        asm volatile("s_nop 3" : "+v"(sc[i - 1]));
        CLIP(i - 1);
        TMAX(sc[i - 1], 0, 8);
        SB();
        mfma_vv(sc[i], kb[i % 3][1], qf[1]);
        if (i + 2 < 8) KRD(i + 2);
        TMAX(sc[i - 1], 8, 16);
        SB();
      }
      unsigned vb = L_V + w * 2048 + tp0;
      OPAQUE(vb);
      v4u vf[3];
      vf[0] = trf(L, vb, vb + 512);
      vf[1] = trf(L, vb + 1024, vb + 1536);
      if (layer == 0) {
        const rsrc_t ro = mkrs(wo, mlp ? DM * DM * 2 : 0);
#pragma unroll 1
        for (int i = 7 * w + 4; i < 7 * w + 7; ++i) dma16(ro, (lane >> 2) * 896 + (lane & 3) * 16, i * 16 * 896 + j * 64, L_WO + i * 1024);
      }
      MFMA_DONE(sc[7]);
      CLIP(7);
      TMAX(sc[7], 0, 16);
      float m = max3(max3(mx[0], mx[1], mx[2]), mx[3], mx[3]);
      m = max3(m, xh(m), m);
      const f32x2 nm = {-m, -m};
      f32x16 o;
#pragma unroll
      for (int st = 0; st < 18; ++st) {
        f32x2 e[4];
#pragma unroll
        for (int d = 0; d < 4; ++d) e[d] = f32x2{sc[st >> 1][8 * (st & 1) + 2 * d], sc[st >> 1][8 * (st & 1) + 2 * d + 1]} + nm;
        v4u pf;
        SB();
#pragma unroll
        for (int d = 0; d < 4; ++d) { e[d][0] = __builtin_amdgcn_exp2f(e[d][0]); e[d][1] = __builtin_amdgcn_exp2f(e[d][1]); }
        SB();
#pragma unroll
        for (int d = 0; d < 4; ++d) pf[d] = pk(e[d]);
        SB();
        if (st + 2 < 18) vf[(st + 2) % 3] = trf(L, vb + (st + 2) * 1024, vb + (st + 2) * 1024 + 512);
        else asm volatile("s_nop 1" : "+v"(pf));
        if (st < 12) { W1PIECE(21 * w + 3 + 3 * (st >> 1) + 2 * (st & 1)); if (!(st & 1)) W1PIECE(21 * w + 4 + 3 * (st >> 1)); }
        SB();
        if (st == 0) mfma_v0(o, vf[0], pf); else mfma_vv(o, vf[st % 3], pf);
        SB();
      }
      MFMA_DONE(o);
      const float other = xh(o[12]);
      const float inv = __builtin_amdgcn_rcpf(h ? o[12] : other);
#pragma unroll
      for (int q = 0; q < 4; ++q) ST8((v2u{pk(f32x2{o[4 * q], o[4 * q + 1]} * inv), pk(f32x2{o[4 * q + 2], o[4 * q + 3]} * inv)}), oaddr[q]);
      STAMP(20); STAMPC(28);
      VMWAIT(0);
      __syncthreads();
      STAMP(5);
      if (w == 0 && lane < 14) __hip_atomic_store(flag_line(ws, layer, E_O, c * 16 + lane) + j, epoch, RLX);
    }
    if (mlp) {
      FRESH();
#pragma unroll 1
      for (int i = 84 + 7 * w; i < 91 + 7 * w; ++i) W1PIECE(i);
      v2u res[4];
#pragma unroll
      for (int q = 0; q < 4; ++q)
        res[q] = layer == 0 ? __builtin_amdgcn_raw_buffer_load_b64(rx, (32 * TT + n) * 896 + j * 64 + 8 * h + 16 * q, 0, 0) : LD8(W_X + TT * XB + 2 * j * 1024 + n * 16 + 8 * h + (q >> 1) * 1024 + (q & 1) * 512);
      if (w == 0) poll((layer == 0 && lane >= 16) ? flag_line(ws, 0, E_W2, wg) + (lane - 16) : flag_line(ws, layer, E_O, wg) + (lane & 15), epoch, dead);
      __syncthreads();
      STAMP(6);
      FRESH();
      {
        f32x16 a[1];
        unsigned bo[1] = {tp0 + L_WO};
        kloop<1, 1024, 512>(L, rws, W_O + TT * XB + lane * 16, bo, a);
        STAMP(7);
        MFMA_DONE(a[0]);
        unsigned o[8];
#pragma unroll
        for (int q = 0; q < 4; ++q) {
          o[2 * q] = pk(f32x2{a[0][4 * q], a[0][4 * q + 1]} + unpk(res[q].x));
          o[2 * q + 1] = pk(f32x2{a[0][4 * q + 2], a[0][4 * q + 3]} + unpk(res[q].y));
        }
        const int xo = W_X1 + TT * XB;
        ST16((v4u{o[0], o[1], o[2], o[3]}), xo + 2 * j * 1024 + lane * 16);
        ST16((v4u{o[4], o[5], o[6], o[7]}), xo + 2 * j * 1024 + 1024 + lane * 16);
        float s = 0.f, q2 = 0.f;
#pragma unroll
        for (int e = 0; e < 8; ++e) { s = dot2(o[e], ONES, s); q2 = dot2(o[e], o[e], q2); }
        s += xh(s); q2 += xh(q2);
        ST8((v2u{__builtin_bit_cast(unsigned, s), __builtin_bit_cast(unsigned, q2)}), h ? W_END : xo + X_ST + j * 256 + n * 8);
      }
      VMWAIT(0);
      __syncthreads();
      STAMP(8);
      if (w == 0 && lane < 14) __hip_atomic_store(flag_line(ws, layer, E_X1, c * 16 + lane) + j, epoch, RLX);
      if (layer == 0) {
        w2range<0, 14>(w2f, rws, W2V);
        f32x16 ca = zero16();
        colsum<4096, 2048>(L, tr_w1(lane, w), 4, ca);
        colsum_put(L, ca, M_CS1 + w * 128, lane);
      }
      if (w == 0) poll(flag_line(ws, layer, E_X1, wg) + (lane < 14 ? lane : 0), epoch, dead);
      __syncthreads();
      STAMP(9); STAMPC(24);
      FRESH();
      {
        v2u st[7];
#pragma unroll
        for (int i = 0; i < 7; ++i) st[i] = LD8(W_X1 + TT * XB + X_ST + (7 * h + i) * 256 + n * 8);
        SB();
        f32x16 a[4];
        unsigned b4[4] = {tr_w1(lane, 0), tr_w1(lane, 1), tr_w1(lane, 2), tr_w1(lane, 3)};
        kloop<4, 4096, 2048>(L, rws, W_X1 + TT * XB + lane * 16, b4, a);
        STAMP(10); STAMPC(25);
        f32x4 cs[4], bb1[4];
#pragma unroll
        for (int q = 0; q < 4; ++q) { cs[q] = *(LAS f32x4*)(L + M_CS1 + q * 32 + h * 16); bb1[q] = *(LAS f32x4*)(L + M_B1 + q * 32 + h * 16); }
        row_stats(st, mean, var);
        float rs = __builtin_amdgcn_rsqf(var + 1e-5f);
        rs = rs * __builtin_amdgcn_rsqf(var * rs * rs + 1e-5f);
        const float nmr = -mean * rs;
        asm volatile("s_nop 7" : "+v"(a[0]), "+v"(a[1]), "+v"(a[2]), "+v"(a[3]));
#pragma unroll
        for (int cc = 0; cc < 4; ++cc) {
          unsigned o[8];
          f32x2 v[8], z[8];
#pragma unroll
          for (int e = 0; e < 8; ++e) z[e] = f32x2{cs[e >> 1][2 * (e & 1)], cs[e >> 1][2 * (e & 1) + 1]} * nmr + f32x2{bb1[e >> 1][2 * (e & 1)], bb1[e >> 1][2 * (e & 1) + 1]};
          SB();
          if (cc < 3) {
#pragma unroll
            for (int q = 0; q < 4; ++q) { cs[q] = *(LAS f32x4*)(L + M_CS1 + (cc + 1) * 128 + q * 32 + h * 16); bb1[q] = *(LAS f32x4*)(L + M_B1 + (cc + 1) * 128 + q * 32 + h * 16); }
          }
          if (layer == 0) { if (cc == 0) w2range<14, 25>(w2f, rws, W2V); if (cc == 1) w2range<25, 36>(w2f, rws, W2V); if (cc == 2) w2range<36, 46>(w2f, rws, W2V); if (cc == 3) w2range<46, 56>(w2f, rws, W2V); }
#pragma unroll
          for (int e = 0; e < 8; ++e) v[e] = f32x2{a[cc][2 * e], a[cc][2 * e + 1]} * rs + z[e];
          SB();
#pragma unroll
          for (int e = 0; e < 8; ++e) z[e] = v[e] * v[e];
          SB();
#pragma unroll
          for (int e = 0; e < 8; ++e) z[e] = z[e] * -0.10294324f - 2.3022082f;
          SB();
#pragma unroll
          for (int e = 0; e < 8; ++e) z[e] *= v[e];
          SB();
#pragma unroll
          for (int e = 0; e < 8; ++e) { z[e][0] = __builtin_amdgcn_exp2f(z[e][0]); z[e][1] = __builtin_amdgcn_exp2f(z[e][1]); }
          SB();
#pragma unroll
          for (int e = 0; e < 8; ++e) z[e] += 1.0f;
          SB();
#pragma unroll
          for (int e = 0; e < 8; ++e) { z[e][0] = __builtin_amdgcn_rcpf(z[e][0]); z[e][1] = __builtin_amdgcn_rcpf(z[e][1]); }
          SB();
#pragma unroll
          for (int e = 0; e < 8; ++e) v[e] *= z[e];
          SB();
#pragma unroll
          for (int e = 0; e < 8; ++e) o[e] = pk(v[e]);
          const int ho = W_H + TT * HB + (8 * j + 2 * cc) * 1024 + lane * 16;
          ST16((v4u{o[0], o[1], o[2], o[3]}), ho);
          ST16((v4u{o[4], o[5], o[6], o[7]}), ho + 1024);
          SB();
          if (cc == 2) {
            if (layer == 0) VMWAIT(12); else VMWAIT(2);
            __builtin_amdgcn_s_barrier();
            if (w == 0 && lane < 14) __hip_atomic_store(flag_line(ws, layer, E_H0, c * 16 + lane) + j, epoch, RLX);
          }
        }
      }
      STAMP(21); STAMPC(26);
      VMWAIT(0);
      __syncthreads();
      STAMP(11);
      if (w == 0 && lane < 14) __hip_atomic_store(flag_line(ws, layer, E_H1, c * 16 + lane) + j, epoch, RLX);
      FRESH();
      const int T5 = 4 * c + 2 * hf5, k5 = 4 * jb + w;
      v2u r5[2][2];
#pragma unroll
      for (int m = 0; m < 2; ++m)
#pragma unroll
        for (int e = 0; e < 2; ++e) r5[m][e] = LD8(W_X1 + (T5 + m) * XB + k5 * 1024 + e * 512 + n * 16 + 8 * h);
      const v4u bb2 = *(const v4u*)(b2 + k5 * 16 + 8 * h);
      if (w == 0) poll(flag_line(ws, layer, E_H0, wg) + (lane < 14 ? lane : 0), epoch, dead);
      __syncthreads();
      STAMP(12); STAMPC(29);
      {
        f32x16 acc[2][2];
        v4u hf[12];
        const unsigned hv = W_H + T5 * HB + w * 1024 + lane * 16;
        auto ldh = [&](int i) {
          const int so = (i < 14 ? 8 * i : 8 * (i - 14) + 4) * 1024;
          hf[2 * (i % 6)] = __builtin_amdgcn_raw_buffer_load_b128(rws, hv, so, 16);
          hf[2 * (i % 6) + 1] = __builtin_amdgcn_raw_buffer_load_b128(rws, hv, so + HB, 16);
        };
        GAS u64* fp1 = flag_line(ws, layer, E_H1, wg) + (lane < 14 ? lane : 0);
        u64 f1;
#pragma unroll
        for (int i = 0; i < 5; ++i) ldh(i);
        SB();
#pragma unroll
        for (int i = 0; i < 28; ++i) {
#pragma unroll
          for (int m = 0; m < 2; ++m) {
#pragma unroll
            for (int x = 0; x < 2; ++x) { if (i == 0) mfma_a0(acc[x][m], w2f[i][x], hf[m]); else mfma_av(acc[x][m], w2f[i][x], hf[2 * (i % 6) + m]); }
            SB();
            if (m == 0 && i == 4) f1 = __hip_atomic_load(fp1, RLX);
            if (m == 0 && i == 9) CHECK(f1, fp1);
            if (m == 0 && i + 5 < 28) ldh(i + 5);
            if (m == 0 && i == 24 && layer == 0) WQ7(14 * w);
            SB();
          }
        }
        STAMP(13); STAMPC(30);
        FRESH();
        asm volatile("s_nop 11" : "+v"(acc[0][0]), "+v"(acc[0][1]), "+v"(acc[1][0]), "+v"(acc[1][1]));
#pragma unroll
        for (int d = 0; d < 4; ++d)
#pragma unroll
          for (int x = 0; x < 2; ++x)
#pragma unroll
            for (int m = 0; m < 2; ++m)
              *(LAS f32x4*)(L + L_RED + w * 4096 + lane * 16 + d * 16384 + (x * 2 + m) * 1024) = f32x4{acc[x][m][4 * d], acc[x][m][4 * d + 1], acc[x][m][4 * d + 2], acc[x][m][4 * d + 3]};
        __syncthreads();
        if (layer == 0) WQ7(14 * w + 7);
        f32x4 p[2][2];
#pragma unroll
        for (int s = 0; s < 4; ++s)
#pragma unroll
          for (int x = 0; x < 2; ++x)
#pragma unroll
            for (int m = 0; m < 2; ++m) { const f32x4 t = *(LAS f32x4*)(L + L_RED + w * 16384 + lane * 16 + s * 4096 + (x * 2 + m) * 1024); p[x][m] = s == 0 ? t : p[x][m] + t; }
#pragma unroll
        for (int m = 0; m < 2; ++m) {
          const unsigned rr[4] = {r5[m][0].x, r5[m][0].y, r5[m][1].x, r5[m][1].y};
          v4u o;
#pragma unroll
          for (int d = 0; d < 4; ++d) o[d] = pk(f32x2{p[d >> 1][m][2 * (d & 1)], p[d >> 1][m][2 * (d & 1) + 1]} + unpk(rr[d]) + unpk(bb2[d]));
          if (layer == 0) {
            ST16(o, W_X + (4 * c + 2 * hf5 + m) * XB + (4 * jb + w) * 1024 + lane * 16);
            float s = 0.f, q2 = 0.f;
#pragma unroll
            for (int e = 0; e < 4; ++e) { s = dot2(o[e], ONES, s); q2 = dot2(o[e], o[e], q2); }
            *(LAS f32x2*)(L + L_RST + ((m * 8 + w * 2 + h) * 32 + n) * 8) = f32x2{s, q2};
          } else {
            *(v4u*)(out + (32 * (4 * c + 2 * hf5 + m) + n) * 448 + (4 * jb + w) * 16 + 8 * h) = o;
          }
        }
      }
    } else if (layer == 1 && wg == 15 && w == 0) {
      bool ok = false;
      for (int spins = 0; spins < SPIN_CAP && !ok; ++spins) {
        bool a = true;
#pragma unroll
        for (int i = 0; i < 4; ++i) a &= __hip_atomic_load((GAS u64*)(ws + W_STARTED) + lane + 64 * i, RLX) == epoch;
        ok = __builtin_amdgcn_ballot_w64(!a) == 0;
      }
      if (lane == 0) __hip_atomic_store((GAS u64*)(ws + W_E), epoch, RLX);
    }
    if (layer == 0) {
      FRESH();
      __syncthreads();
      if (mlp && w < 2) {
        f32x2 t = {0.f, 0.f};
#pragma unroll
        for (int i = 0; i < 4; ++i) t += *(LAS f32x2*)(L + L_RST + ((h * 8 + 4 * w + i) * 32 + n) * 8);
        ST8(__builtin_bit_cast(v2u, t), W_X + (4 * c + 2 * hf5 + h) * XB + X_ST + (2 * jb + w) * 256 + n * 8);
      }
      VMWAIT(0);
      __syncthreads();
      STAMP(14);
      if (w == 0 && mlp && lane < 16) __hip_atomic_store(flag_line(ws, 1, E_X, c * 16 + lane) + j, epoch, RLX);
      if (!mlp) {
        poll(flag_line(ws, 0, E_WQ, wg) + (lane & 15), epoch, dead);
        WQ7(14 * w); WQ7(14 * w + 7);
      }
      WQ7(56 + 7 * w);
      if (w == 0) poll(flag_line(ws, 1, E_X, wg) + (lane < 14 ? lane : 0), epoch, dead);
      VMWAIT(0);
      __syncthreads();
      STAMP(15);
    }
    asm volatile("" : "=v"(qf[0]), "=v"(qf[1]), "=v"(kf[0]), "=v"(kf[1]));
    STAMP(16);
  }
}
}

extern "C" void kernel_launch(void* const* d_in, const int* in_sizes, int n_in, void* d_out, int out_size, void* d_ws, size_t ws_size, hipStream_t stream) {
  (void)hipFuncSetAttribute((const void*)mk::fused, hipFuncAttributeMaxDynamicSharedMemorySize, mk::L_TOTAL);
  mk::fused<<<256, 256, mk::L_TOTAL, stream>>>((const unsigned short*)d_in[0], (const unsigned short*)d_in[1], (const unsigned short*)d_in[2],
      (const unsigned short*)d_in[4], (const unsigned short*)d_in[5], (const unsigned short*)d_in[6], (const unsigned short*)d_in[7],
      (unsigned short*)d_out, (unsigned char*)d_ws);
}
```

```cpp
#include <hip/hip_runtime.h>
#include <stdint.h>

namespace mk {
typedef unsigned v4u __attribute__((ext_vector_type(4)));
typedef unsigned v2u __attribute__((ext_vector_type(2)));
typedef float f32x4 __attribute__((ext_vector_type(4)));
typedef float f32x2 __attribute__((ext_vector_type(2)));
typedef float f32x16 __attribute__((ext_vector_type(16)));
typedef short s16x4 __attribute__((ext_vector_type(4)));
typedef __bf16 bf16x8 __attribute__((ext_vector_type(8)));
typedef __bf16 bf16x2 __attribute__((ext_vector_type(2)));
typedef unsigned long long u64;
typedef __amdgpu_buffer_rsrc_t rsrc_t;
#define LAS __attribute__((address_space(3)))
#define GAS __attribute__((address_space(1)))
#define RLX __ATOMIC_RELAXED, __HIP_MEMORY_SCOPE_AGENT
#define DI __device__ __forceinline__
#define SB() __builtin_amdgcn_sched_barrier(0)
#define OPAQUE(x) asm volatile("" : "+v"(x))

constexpr int SEQ = 2048, DM = 448, FF = 1792;
constexpr int U = 28672;
constexpr int L_K = 3 * U, L_V = L_K + 22 * 1024, L_WO = L_K + 46 * 1024, L_MISC = L_WO + U, L_TOTAL = 163840;
constexpr int L_RED = 2 * U, L_RST = L_RED + 65536;
constexpr int M_CSQ = L_MISC + 256, M_CS1 = M_CSQ + 384, M_B1 = M_CS1 + 512;
constexpr int L_XS = 2 * U;
constexpr int L_CSP = M_CS1;
static_assert(L_MISC + 2048 == L_TOTAL && L_XS + 4 * 24576 <= L_MISC, "LDS map");
constexpr int W_E = 0, W_STARTED = 4096, W_FLAG = 8192;
enum { E_KV = 0  , E_O = 4, E_X1 = 5, E_H0 = 6, E_H1 = 7, E_X = 8, E_W2 = 9  , E_WQ = 11, NEDGE = 12 };
constexpr int W_KV = 1 << 20, KV_L = 16 * 64 * 4096;
constexpr int XB = 32 * 1024, X_L = 64 * XB;
constexpr int X_ST = 28 * 1024;
constexpr int W_O = W_KV + 2 * KV_L, W_X1 = W_O + X_L, W_X = W_X1 + X_L, W_H = W_X + X_L;
constexpr int HB = 112 * 1024, H_L = 64 * HB;
constexpr int W_W2T = W_H + H_L;
constexpr int W_WQT = W_W2T + 7 * 112 * 2048;
constexpr int W_END = W_WQT + 16 * 3 * U;
static_assert(W_FLAG + 2 * 256 * NEDGE * 128 <= W_KV, "flags");
constexpr int SPIN_CAP = 1 << 16;
constexpr int KV1 = 8, PACE = 2;
constexpr int PW = 5, OR_ = 8;
constexpr int CODE_LINES = 438;
constexpr unsigned ONES = 0x3f803f80u;

template <int V> struct IC { static constexpr int value = V; };
DI f32x16 mfma(v4u a, v4u b, f32x16 c) {
  return __builtin_amdgcn_mfma_f32_32x32x16_bf16(__builtin_bit_cast(bf16x8, a), __builtin_bit_cast(bf16x8, b), c, 0, 0, 0);
}
DI void mfma_v0(f32x16& acc, v4u a, v4u b) { asm volatile("v_mfma_f32_32x32x16_bf16 %0, %1, %2, 0" : "=&v"(acc) : "v"(a), "v"(b)); }
DI void mfma_vv(f32x16& acc, v4u a, v4u b) { asm volatile("v_mfma_f32_32x32x16_bf16 %0, %1, %2, %0" : "+v"(acc) : "v"(a), "v"(b)); }
DI void mfma_a0(f32x16& acc, v4u a, v4u b) { asm volatile("v_mfma_f32_32x32x16_bf16 %0, %1, %2, 0" : "=&v"(acc) : "a"(a), "v"(b)); }
DI void mfma_av(f32x16& acc, v4u a, v4u b) { asm volatile("v_mfma_f32_32x32x16_bf16 %0, %1, %2, %0" : "+v"(acc) : "a"(a), "v"(b)); }
#define MFMA_DONE(x) asm volatile("s_nop 11" : "+v"(x))
DI v2u trd(LAS unsigned char* L, unsigned a) {
  return __builtin_bit_cast(v2u, __builtin_amdgcn_ds_read_tr16_b64_v4i16((LAS s16x4*)(L + a)));
}
DI v4u trf(LAS unsigned char* L, unsigned a0, unsigned a1) { const v2u p = trd(L, a0), q = trd(L, a1); return v4u{p.x, p.y, q.x, q.y}; }
DI unsigned pk(float a, float b) { f32x2 f = {a, b}; return __builtin_bit_cast(unsigned, __builtin_convertvector(f, bf16x2)); }
DI unsigned pk(f32x2 f) { return __builtin_bit_cast(unsigned, __builtin_convertvector(f, bf16x2)); }
DI float blo(unsigned u) { return __builtin_bit_cast(float, u << 16); }
DI float bhi(unsigned u) { return __builtin_bit_cast(float, u & 0xffff0000u); }
DI f32x2 unpk(unsigned u) { return f32x2{blo(u), bhi(u)}; }
DI int rfl(int x) { return __builtin_amdgcn_readfirstlane(x); }
DI float dot2(unsigned a, unsigned b, float c) { return __builtin_amdgcn_fdot2_f32_bf16(__builtin_bit_cast(bf16x2, a), __builtin_bit_cast(bf16x2, b), c, false); }
DI float max3(float a, float b, float c) { float r; asm("v_max3_f32 %0, %1, %2, %3" : "=v"(r) : "v"(a), "v"(b), "v"(c)); return r; }
DI float xh(float v) {
  const unsigned u = __builtin_bit_cast(unsigned, v);
  const auto r = __builtin_amdgcn_permlane32_swap(u, u, false, false);
  return __builtin_bit_cast(float, (threadIdx.x & 32) ? r[0] : r[1]);
}
DI rsrc_t mkrs(const void* p, int bytes) { return __builtin_amdgcn_make_buffer_rsrc((void*)p, 0, bytes, 0x00020000); }
DI void dma16(rsrc_t rs, unsigned voff, unsigned soff, unsigned ldst) {
  asm volatile("s_mov_b32 m0, %0\n\ts_nop 0\n\tbuffer_load_dwordx4 %1, %2, %3 offen lds" :: "s"(ldst), "v"(voff), "s"(rs), "s"(soff) : "memory");
}
template <int N> DI void dmaN(rsrc_t rs, unsigned voff, unsigned soff, unsigned ldst) {
  if constexpr (N == 4)
    asm volatile("s_mov_b32 m0, %0\n\ts_nop 0\n\tbuffer_load_dwordx4 %1, %2, %3 offen sc1 lds\n\tbuffer_load_dwordx4 %1, %2, %3 offen offset:1024 sc1 lds\n\t"
                 "buffer_load_dwordx4 %1, %2, %3 offen offset:2048 sc1 lds\n\tbuffer_load_dwordx4 %1, %2, %3 offen offset:3072 sc1 lds" :: "s"(ldst), "v"(voff), "s"(rs), "s"(soff) : "memory");
  else
    asm volatile("s_mov_b32 m0, %0\n\ts_nop 0\n\tbuffer_load_dwordx4 %1, %2, %3 offen sc1 lds\n\tbuffer_load_dwordx4 %1, %2, %3 offen offset:1024 sc1 lds\n\t"
                 "buffer_load_dwordx4 %1, %2, %3 offen offset:2048 sc1 lds" :: "s"(ldst), "v"(voff), "s"(rs), "s"(soff) : "memory");
}
#define VMWAIT(n) asm volatile("s_waitcnt vmcnt(" #n ")" ::: "memory")
#define ST16(v, off) __builtin_amdgcn_raw_buffer_store_b128(v, rws, off, 0, 16)
#define ST8(v, off) __builtin_amdgcn_raw_buffer_store_b64(v, rws, off, 0, 16)
#define LD8(off) __builtin_amdgcn_raw_buffer_load_b64(rws, off, 0, 16)
DI unsigned tr_base(int lane, int tp, bool nat, int par) {
  const int h = lane >> 5, cb = (lane >> 4) & 1, q = (lane >> 2) & 3, p = lane & 3;
  const int row = nat ? 8 * h + 4 * tp + q : 8 * tp + 4 * h + q;
  return row * 64 + ((4 * cb + p + par) & 7) * 8;
}
template <int I> DI void xload(v4u& x, rsrc_t rs, unsigned voff, unsigned s0, unsigned s1) {
  asm volatile("buffer_load_dwordx4 %0, %1, %2, %3 offen offset:%4 sc1" : "=v"(x) : "v"(voff), "s"(rs), "s"(I < 4 ? s0 : s1), "i"((I & 3) * 1024) : "memory");
}
template <int I> DI void w2load(v4u (&w2f)[28][2], rsrc_t rs, unsigned voff) {
  asm volatile("buffer_load_dwordx4 %0, %1, %2, %3 offen offset:%4 sc1" : "=a"(w2f[I >> 1][I & 1]) : "v"(voff), "s"(rs),
               "s"(((I >> 1) < 14 ? 8 * (I >> 1) : 8 * ((I >> 1) - 14) + 4) * 2048), "i"((I & 1) * 1024) : "memory");
}
template <int LO, int HI> DI void w2range(v4u (&w2f)[28][2], rsrc_t rs, unsigned voff) {
  if constexpr (LO < HI) { w2load<LO>(w2f, rs, voff); w2range<LO + 1, HI>(w2f, rs, voff); }
}
DI unsigned tr_w1(int lane, int cc) {
  const int h = lane >> 5, cb = (lane >> 4) & 1, q = (lane >> 2) & 3, p = lane & 3;
  return (4 * h + q) * 256 + ((cc ^ q) * 64) + (4 * cb + p) * 8;
}
template <int I> DI void xfirst(v4u (&xr)[7], rsrc_t rs, unsigned voff) { if constexpr (I < 7) { xload<I>(xr[I], rs, voff, 0u, 4096u); xfirst<I + 1>(xr, rs, voff); } }
template <bool RIDE> constexpr int w2cnt(int s) { return RIDE ? s + (s + 1) / 2 : 0; }
template <bool RIDE, int S> DI void k4step(LAS unsigned char* L, v4u (&xr)[7], rsrc_t rs, unsigned voff, unsigned (&b)[4], v4u (&wb)[4], f32x16 (&acc)[4], v4u (&w2f)[28][2], unsigned w2v) {
  if constexpr (S < 28) {
    constexpr int lo = S < 7 ? 0 : S - 6;
    constexpr int younger = (w2cnt<RIDE>(S) - w2cnt<RIDE>(lo)) + (S < 7 ? 6 : (S <= 21 ? 6 : 27 - S));
    asm volatile("s_waitcnt vmcnt(%1)" : "+v"(xr[S % 7]) : "i"(younger) : "memory");
    SB();
    if constexpr (S == 13) {
#pragma unroll
      for (int q = 0; q < 4; ++q) b[q] += 14 * 4096;
    }
#pragma unroll
    for (int q = 0; q < 4; ++q) {
      if (S == 0) mfma_v0(acc[q], wb[q], xr[0]); else mfma_vv(acc[q], wb[q], xr[S % 7]);
      SB();
      constexpr int o = (S + 1 - (S >= 13 ? 14 : 0)) * 4096;
      if constexpr (S < 27) wb[q] = trf(L, b[q] + o, b[q] + o + 2048);
      if constexpr (RIDE) { if (q == 0) w2load<w2cnt<RIDE>(S)>(w2f, rs, w2v); }
      if constexpr (RIDE && (S & 1) == 0) { if (q == 2) w2load<w2cnt<RIDE>(S) + 1>(w2f, rs, w2v); }
      if constexpr (S + 7 < 28) { if (q == 3) xload<((S + 7) & 3)>(xr[S % 7], rs, voff, ((S + 7) >> 2) * 4096u, 0u); }
      SB();
    }
    k4step<RIDE, S + 1>(L, xr, rs, voff, b, wb, acc, w2f, w2v);
  }
}
template <int S> DI void ostep(LAS unsigned char* L, v4u (&xr)[OR_], rsrc_t rs, unsigned voff, unsigned b, v4u (&wb)[4], f32x16& acc) {
  if constexpr (S < 32) {
    asm volatile("s_waitcnt vmcnt(%1)" : "+v"(xr[S % OR_]) : "i"(S + OR_ <= 32 ? OR_ - 1 : 31 - S) : "memory");
    SB();
    if (S == 0) mfma_v0(acc, wb[0], xr[0]); else mfma_vv(acc, wb[S & 3], xr[S % OR_]);
    SB();
    constexpr int o = ((S + 4) >> 1) * 1792 + (S & 1) * 1024;
    if constexpr (S + 4 < 32) wb[S & 3] = trf(L, b + o, b + o + 512);
    if constexpr (S + OR_ < 32) xload<((S + OR_) & 3)>(xr[S % OR_], rs, voff, ((S + OR_) >> 2) * 4096u, 0u);
    SB();
    ostep<S + 1>(L, xr, rs, voff, b, wb, acc);
  }
}
template <int I> DI void ofirst(v4u (&xr)[OR_], rsrc_t rs, unsigned voff) { if constexpr (I < OR_) { xload<(I & 3)>(xr[I], rs, voff, (I >> 2) * 4096u, 0u); ofirst<I + 1>(xr, rs, voff); } }
DI void oloop(LAS unsigned char* L, rsrc_t rs, unsigned voff, unsigned b, f32x16& acc) {
  v4u xr[OR_], wb[4];
  ofirst<0>(xr, rs, voff);
  OPAQUE(b);
#pragma unroll
  for (int i = 0; i < 4; ++i) wb[i] = trf(L, b + (i >> 1) * 1792 + (i & 1) * 1024, b + (i >> 1) * 1792 + (i & 1) * 1024 + 512);
  ostep<0>(L, xr, rs, voff, b, wb, acc);
}
constexpr int LA = 8;
template <int I> DI void xl(v4u& x, rsrc_t rs, unsigned voff) {
  asm volatile("buffer_load_dwordx4 %0, %1, %2, %3 offen offset:%4 sc1" : "=v"(x) : "v"(voff), "s"(rs), "s"((I >> 2) * 4096), "i"((I & 3) * 1024) : "memory");
}
template <int NC, bool LOAD, int S, class F> DI void krstep(LAS unsigned char* L, v4u (&xf)[28], unsigned b, f32x16* acc, v4u (&wb)[4 / NC][NC], rsrc_t rs, unsigned voff, F bg) {
  constexpr int PD = 4 / NC;
  if constexpr (S < 28) {
    if constexpr (LOAD) asm volatile("s_waitcnt vmcnt(%1)" : "+v"(xf[S]) : "i"(S + LA <= 28 ? LA - 1 : 27 - S) : "memory");
#pragma unroll
    for (int q = 0; q < NC; ++q) {
      SB();
      if (S == 0) mfma_v0(acc[q], wb[0][q], xf[0]); else mfma_vv(acc[q], wb[S % PD][q], xf[S]);
      SB();
      if constexpr (S + PD < 28) wb[S % PD][q] = trf(L, b + q * U + (S + PD) * 1024, b + q * U + (S + PD) * 1024 + 256);
      if (q == 0) { if constexpr (LOAD && S + LA < 28) xl<S + LA>(xf[S + LA], rs, voff); bg(S); }
    }
    krstep<NC, LOAD, S + 1>(L, xf, b, acc, wb, rs, voff, bg);
  }
}
template <int I> DI void xlfirst(v4u (&xf)[28], rsrc_t rs, unsigned voff) { if constexpr (I < LA) { xl<I>(xf[I], rs, voff); xlfirst<I + 1>(xf, rs, voff); } }
template <int NC, bool LOAD, class F> DI void kregs(LAS unsigned char* L, v4u (&xf)[28], unsigned b, f32x16* acc, rsrc_t rs, unsigned voff, F bg) {
  constexpr int PD = 4 / NC;
  v4u wb[PD][NC];
  if constexpr (LOAD) xlfirst<0>(xf, rs, voff);
  OPAQUE(b);
#pragma unroll
  for (int s = 0; s < PD; ++s)
#pragma unroll
    for (int q = 0; q < NC; ++q) wb[s][q] = trf(L, b + q * U + s * 1024, b + q * U + s * 1024 + 256);
  krstep<NC, LOAD, 0>(L, xf, b, acc, wb, rs, voff, bg);
}
template <int NI, int I0, int I1> DI void colpart(LAS unsigned char* L, unsigned b, f32x16 (&a)[NI], unsigned dst, int lane) {
  v4u ones = {ONES, ONES, ONES, ONES}, f[NI][I1 - I0];
  OPAQUE(b);
  asm volatile("" : "+v"(ones));
#pragma unroll
  for (int i = I0; i < I1; ++i)
#pragma unroll
    for (int q = 0; q < NI; ++q) f[q][i - I0] = trf(L, b + q * U + i * 1024, b + q * U + i * 1024 + 256);
  SB();
#pragma unroll
  for (int i = I0; i < I1; ++i)
#pragma unroll
    for (int q = 0; q < NI; ++q) { if (i == 0) mfma_v0(a[q], f[q][0], ones); else mfma_vv(a[q], f[q][i - I0], ones); }
  if constexpr (I1 == 7) {
#pragma unroll
    for (int q = 0; q < NI; ++q) {
      if (q == 0) MFMA_DONE(a[NI - 1]);
      asm volatile("" : "+v"(a[q]));
      if ((lane & 31) == 0) {
#pragma unroll
        for (int e = 0; e < 4; ++e) *(LAS f32x4*)(L + dst + q * 512 + e * 32 + (lane >> 5) * 16) = f32x4{a[q][4 * e], a[q][4 * e + 1], a[q][4 * e + 2], a[q][4 * e + 3]};
      }
    }
  }
}
template <int KS, int TR1> DI void colsum(LAS unsigned char* L, unsigned b, int rounds, f32x16& a) {
  const v4u ones = {ONES, ONES, ONES, ONES};
#pragma unroll 1
  for (int s = 0; s < rounds; ++s) {
    OPAQUE(b);
    v4u f[7];
#pragma unroll
    for (int i = 0; i < 7; ++i) f[i] = trf(L, b + i * KS, b + i * KS + TR1);
    SB();
#pragma unroll
    for (int i = 0; i < 7; ++i) a = mfma(f[i], ones, a);
    b += 7 * KS;
  }
}
DI void colsum_put(LAS unsigned char* L, const f32x16& a, unsigned dst, int lane) {
  if ((lane & 31) == 0) {
#pragma unroll
    for (int q = 0; q < 4; ++q) *(LAS f32x4*)(L + dst + q * 32 + (lane >> 5) * 16) = f32x4{a[4 * q], a[4 * q + 1], a[4 * q + 2], a[4 * q + 3]};
  }
}
DI f32x16 zero16() { f32x16 a;
#pragma unroll
  for (int r = 0; r < 16; ++r) a[r] = 0.f;
  return a; }
DI GAS u64* flag_line(unsigned char* ws, int layer, int edge, int wg) { return (GAS u64*)(ws + W_FLAG + ((layer * 256 + wg) * NEDGE + edge) * 128); }
DI void poll(GAS u64* p, u64 epoch, bool& dead) {
  if (dead) return;
  for (int spins = 0;; ++spins) {
    const u64 f = __hip_atomic_load(p, RLX);
    if (__builtin_amdgcn_ballot_w64(f != epoch) == 0) break;
    if (spins > SPIN_CAP) { dead = true; break; }
  }
  __builtin_amdgcn_fence(__ATOMIC_ACQUIRE, "wavefront");
}
#define CHECK(f, p) do { if (__builtin_amdgcn_ballot_w64((f) != epoch) != 0) poll(p, epoch, dead); __builtin_amdgcn_fence(__ATOMIC_ACQUIRE, "wavefront"); } while (0)
DI void put_k(LAS unsigned char* L, int lt, int lane, v4u a, v4u b) {
  *(LAS v4u*)(L + L_K + lt * 2048 + lane * 16) = a;
  *(LAS v4u*)(L + L_K + lt * 2048 + 1024 + lane * 16) = b;
}
DI void put_v(LAS unsigned char* L, int lt, int lane, v4u a, v4u b) {
  const unsigned o = L_V + lt * 2048 + (lane & 31) * 64 + (lane >> 5) * 8;
  *(LAS v2u*)(L + o) = v2u{a.x, a.y};
  *(LAS v2u*)(L + o + 16) = v2u{a.z, a.w};
  *(LAS v2u*)(L + o + 32) = v2u{b.x, b.y};
  *(LAS v2u*)(L + o + 48) = v2u{b.z, b.w};
}
DI void ln_pack(LAS unsigned char* L, const f32x16& a, unsigned cs, int h, float nmsc, float sc, float pad0, v4u& o0, v4u& o1) {
  unsigned o[8];
  f32x4 cq[4];
  f32x2 t[8];
#pragma unroll
  for (int q = 0; q < 4; ++q) cq[q] = *(LAS f32x4*)(L + cs + q * 32 + h * 16);
#pragma unroll
  for (int e = 0; e < 8; ++e) t[e] = f32x2{a[2 * e], a[2 * e + 1]} * sc;
  SB();
#pragma unroll
  for (int e = 0; e < 8; ++e) t[e] += f32x2{cq[e >> 1][2 * (e & 1)], cq[e >> 1][2 * (e & 1) + 1]} * nmsc;
  SB();
#pragma unroll
  for (int e = 0; e < 8; ++e) o[e] = pk(t[e]);
  if (h == 1) { o[6] = pk(pad0, 0.f); o[7] = 0u; }
  o0 = v4u{o[0], o[1], o[2], o[3]}; o1 = v4u{o[4], o[5], o[6], o[7]};
}
DI void row_stats(const v2u (&st)[7], float& mean, float& var) {
  f32x2 t = __builtin_bit_cast(f32x2, st[0]);
#pragma unroll
  for (int i = 1; i < 7; ++i) t += __builtin_bit_cast(f32x2, st[i]);
  mean = (t[0] + xh(t[0])) * (1.0f / DM);
  var = fmaxf((t[1] + xh(t[1])) * (1.0f / DM) - mean * mean, 0.f);
}

__global__ void __launch_bounds__(256, 1) fused(const unsigned short* __restrict__ xin, const unsigned short* __restrict__ wqkv, const unsigned short* __restrict__ wo,
    const unsigned short* __restrict__ w1, const unsigned short* __restrict__ b1, const unsigned short* __restrict__ w2, const unsigned short* __restrict__ b2,
    unsigned short* __restrict__ out, unsigned char* ws) {
  extern __shared__ __attribute__((aligned(16))) unsigned char smem[];
  LAS unsigned char* L = (LAS unsigned char*)smem;
  int lane = threadIdx.x & 63, w = rfl(threadIdx.x >> 6), n = lane & 31, h = lane >> 5;
  int c = 2 * (blockIdx.x & 7) + (blockIdx.x >> 7), j = (blockIdx.x >> 3) & 15;
  const bool mlp = j < 14;
  const int c0_ = c, j0_ = j, w0_ = w, lane0_ = lane;
#define wg (c * 16 + j)
#define TT (4 * c + w)
#define hf5 (j >= 7 ? 1 : 0)
#define jb (j >= 7 ? j - 7 : j)
#define W2V (W_W2T + (jb * 112 + w) * 2048 + lane * 16)
#define W1PIECE(i) dma16(rw1, (lane >> 4) * 3584 + (((lane & 15) ^ ((lane >> 4) << 2)) * 16), (i) * 4 * 3584 + j * 256, (i) * 1024)
#define WQ7(i) do { dmaN<4>(rws, lane * 16, W_WQT + j * 3 * U + (i) * 1024, (i) * 1024); dmaN<3>(rws, lane * 16, W_WQT + j * 3 * U + ((i) + 4) * 1024, ((i) + 4) * 1024); } while (0)
#define FRESH() do { c = c0_; j = j0_; w = w0_; lane = lane0_; asm volatile("" : "+s"(c), "+s"(j), "+s"(w)); asm volatile("" : "+v"(lane)); n = lane & 31; h = lane >> 5; } while (0)
  const rsrc_t rx = mkrs(xin, SEQ * DM * 2), rq = mkrs(wqkv, DM * 3 * DM * 2), rw1 = mkrs(w1, mlp ? DM * FF * 2 : 0);
  unsigned ctouch;
  {
    const unsigned char* pc = (const unsigned char*)__builtin_amdgcn_s_getpc();
    const int part = (j >> 2) + 4 * (c & 1);
    asm volatile("global_load_dword %0, %1, off" : "=v"(ctouch) : "v"(pc + ((w == (j & 3) && part * 64 + lane < CODE_LINES) ? part * 8192 + lane * 128 : 0)) : "memory");
  }
  v4u xf[28];
  {
    const unsigned vo = (32 * TT + (lane >> 3)) * 896;
    int cb = j >= 12 ? j - 12 : (j >= 6 ? j - 6 : j);
#pragma unroll 1
    for (int t = 0; t < 6; ++t) {
#pragma unroll
      for (int r = 0; r < 4; ++r) dma16(rx, vo + (((lane & 7) ^ (((lane >> 4) + 4 * r) & 7)) * 16), cb * 128 + r * 8 * 896, L_XS + w * 24576 + cb * 4096 + r * 1024);
      cb = cb == 5 ? 0 : cb + 1;
    }
#pragma unroll
    for (int i = 24; i < 28; ++i) asm volatile("buffer_load_dwordx4 %0, %1, %2, 0 offen offset:%3" : "=v"(xf[i]) : "v"((32 * TT + n) * 896 + 16 * h), "s"(rx), "i"(32 * i) : "memory");
    const unsigned vq = (lane >> 2) * 2688 + (lane & 3) * 16;
    const int i0 = 7 * ((w + 2 * (c & 1)) & 3);
#pragma unroll
    for (int i = 0; i < KV1; ++i) dma16(rq, vq, (i0 + (i >> 1)) * 16 * 2688 + 896 + (i & 1) * 896 + ((56 * j) & ~15), (i & 1) * U + (i0 + (i >> 1)) * 1024);
  }
  unsigned touch;
  {
    const unsigned char* p = ws + 4096 + (size_t)(lane & 15) * (2u << 20);
    if (lane >= 16) p = (const unsigned char*)w1 + (7 * c >> 2) * 16 * 3584;
    if (lane == 17) p = (const unsigned char*)w2 + 7 * c * 16 * 896;
    if (lane == 18) p = (const unsigned char*)wo + c * 16 * 896;
    if (lane == 19) p = (const unsigned char*)b1;
    if (lane == 20) p = (const unsigned char*)b2;
    if (lane == 21) p = (const unsigned char*)out + c * 128 * 896;
    asm volatile("global_load_dword %0, %1, off sc1" : "=v"(touch) : "v"(p) : "memory");
  }
  const rsrc_t rws = mkrs(ws, W_END);
  const u64 epoch = __hip_atomic_load((GAS u64*)(ws + W_E), RLX) + 1;
  bool dead = false;
  v4u qf[2], kf[2];
  u64 fkv;
  v2u res0[4], wo1;
  {
      float mean, var;
      f32x16 a[3];
      const unsigned tq = tr_base(lane, 0, true, j & 1);
        asm volatile("s_waitcnt vmcnt(%4)" : "+v"(xf[24]), "+v"(xf[25]), "+v"(xf[26]), "+v"(xf[27]) : "i"(KV1 + 1) : "memory");
        const unsigned xb = L_XS + w * 24576 + n * 128;
#pragma unroll
        for (int i = 0; i < 24; ++i) xf[i] = *(LAS v4u*)(L + xb + (((2 * (i & 3) + h) ^ ((n >> 1) & 7)) * 16) + (i >> 2) * 4096);
        float s[2] = {0.f, 0.f}, q[2] = {0.f, 0.f};
#pragma unroll
        for (int i = 0; i < 28; ++i) {
          if (i % PACE == 0 && KV1 + i / PACE < 14) {
            const int p = KV1 + i / PACE, i1 = 7 * ((w + 2 * (c & 1)) & 3) + (p >> 1);
            SB();
            dma16(rq, (lane >> 2) * 2688 + (lane & 3) * 16, i1 * 16 * 2688 + 896 + (p & 1) * 896 + ((56 * j) & ~15), (p & 1) * U + i1 * 1024);
            SB();
          }
#pragma unroll
          for (int e = 0; e < 4; ++e) { s[e & 1] = dot2(xf[(i + 24) % 28][e], ONES, s[e & 1]); q[e & 1] = dot2(xf[(i + 24) % 28][e], xf[(i + 24) % 28][e], q[e & 1]); }
        }
        mean = s[0] + s[1]; var = q[0] + q[1];
        mean = (mean + xh(mean)) * (1.0f / DM);
        var = fmaxf((var + xh(var)) * (1.0f / DM) - mean * mean, 0.f);
        {
          v4u ra = xf[0], rb = xf[1];
#pragma unroll
          for (int i = 1; i < 14; ++i)
            if (j == i) { asm volatile("" ::: "memory"); ra = xf[2 * i]; rb = xf[2 * i + 1]; }
          const auto s0 = __builtin_amdgcn_permlane32_swap(ra.x, ra.z, false, false), s1 = __builtin_amdgcn_permlane32_swap(ra.y, ra.w, false, false);
          const auto s2 = __builtin_amdgcn_permlane32_swap(rb.x, rb.z, false, false), s3 = __builtin_amdgcn_permlane32_swap(rb.y, rb.w, false, false);
          res0[0] = v2u{s0[0], s1[0]}; res0[1] = v2u{s0[1], s1[1]}; res0[2] = v2u{s2[0], s3[0]}; res0[3] = v2u{s2[1], s3[1]};
        }
        f32x16 cp[2];
        asm volatile("s_waitcnt vmcnt(%2)" : "+v"(mean), "+v"(res0[0]) : "i"(14 - KV1) : "memory");
        colpart<2, 0, KV1 / 2>(L, tq + 7 * ((w + 2 * (c & 1)) & 3) * 1024, cp, 0u, lane);
        asm volatile("s_waitcnt vmcnt(0)" : "+v"(touch), "+v"(ctouch), "+v"(mean), "+v"(var), "+v"(res0[0]), "+v"(res0[3]) :: "memory");
        colpart<2, KV1 / 2, 7>(L, tq + 7 * ((w + 2 * (c & 1)) & 3) * 1024, cp, L_CSP + w * 128, lane);
        __syncthreads();
        const unsigned vq = (lane >> 2) * 2688 + (lane & 3) * 16;
        const int i0 = 7 * ((w + 2 * (c & 1)) & 3);
        const rsrc_t rw2 = mkrs(w2, mlp ? FF * DM * 2 : 0);
        const int so = ((7 * c + 4 * hf5 + w) * 16) * 896 + jb * 128;
        auto bg = [&](int s) {
          if (s < 21 && s % 3 == 0) dma16(rq, vq, (i0 + s / 3) * 16 * 2688 + ((56 * j) & ~15), 2 * U + (i0 + s / 3) * 1024);
        };
        if (threadIdx.x == 0) __hip_atomic_store((GAS u64*)(ws + W_STARTED) + wg, epoch, RLX);
        SB();
        kregs<2, false>(L, xf, tq, a, rws, 0u, bg);
        if (lane < 16) {
          const unsigned o = (lane >> 3) * 512 + (lane & 7) * 16;
          *(LAS f32x4*)(L + M_CSQ + lane * 16) = (*(LAS f32x4*)(L + L_CSP + o) + *(LAS f32x4*)(L + L_CSP + o + 128)) + (*(LAS f32x4*)(L + L_CSP + o + 256) + *(LAS f32x4*)(L + L_CSP + o + 384));
        }
      const float rs = __builtin_amdgcn_rsqf(var + 1e-5f);
      asm volatile("s_nop 7" : "+v"(a[0]), "+v"(a[1]));
      const int kvoff = W_KV + 0 * KV_L + (j * 64 + TT) * 4096 + lane * 16;
      v4u v0, v1;
      ln_pack(L, a[0], M_CSQ, h, -mean * rs, rs, 0.f, kf[0], kf[1]);
      ST16(kf[0], kvoff); ST16(kf[1], kvoff + 1024);
      ln_pack(L, a[1], M_CSQ + 128, h, -mean * rs, rs, 1.f, v0, v1);
      ST16(v0, kvoff + 2048); ST16(v1, kvoff + 3072);
      if (w < 3) put_k(L, 8 + w, lane, kf[0], kf[1]);
      put_v(L, 8 + w, lane, v0, v1);
      {
        dma16(rw2, (lane >> 3) * 896 + (lane & 7) * 16, so, L_WO + w * 2048);
        dma16(rw2, (lane >> 3) * 896 + (lane & 7) * 16, so + 8 * 896, L_WO + w * 2048 + 1024);
        wo1.x = __builtin_amdgcn_raw_buffer_load_b32(mkrs(b1, mlp ? FF * 2 : 0), 256 * j + 4 * lane, 0, 0);
        wo1.y = __builtin_amdgcn_raw_buffer_load_b32(rws, W_FLAG + (((lane >> 4) & 1) * 256 + wg) * NEDGE * 128 + (lane & 15) * 128, 0, 16);
        SB();
        VMWAIT(8);
        __syncthreads();
        { f32x16 cq[1]; colpart<1, 0, 7>(L, tq + 2 * U + 7 * w * 1024, cq, L_CSP + w * 128, lane); }
        kregs<1, false>(L, xf, tq + 2 * U, a + 2, rws, 0u, [&](int s) {
          if (s == 2) {
            VMWAIT(4);
            if (lane < 2 && c + 1 + lane < 16) __hip_atomic_store(flag_line(ws, 0, E_KV + (lane ? 0 : 2) + (w >> 1), wg + 16 * (1 + lane)) + (w & 1), epoch, RLX);
          }
          if (s == 18) {
            const int wi = w < 3 ? w : 0;
            ST16(*(LAS v4u*)(L + wi * U + c * 1024 + lane * 16), w < 3 ? W_WQT + (j * 3 + w) * U + c * 1024 + lane * 16 : W_END);
            ST16(*(LAS v4u*)(L + wi * U + ((c + 16) & 15) * 1024 + lane * 16 + (c < 12 ? 16384 : 0)), (w < 3 && c < 12) ? W_WQT + (j * 3 + w) * U + (c + 16) * 1024 + lane * 16 : W_END);
          }
        });
        if (w == 2) *(LAS unsigned*)(L + L_MISC + lane * 4) = 0u;
        __syncthreads();
        if (lane < 8) {
          const unsigned o = lane * 16;
          *(LAS f32x4*)(L + M_CSQ + 256 + lane * 16) = (*(LAS f32x4*)(L + L_CSP + o) + *(LAS f32x4*)(L + L_CSP + o + 128)) + (*(LAS f32x4*)(L + L_CSP + o + 256) + *(LAS f32x4*)(L + L_CSP + o + 384));
        }
        MFMA_DONE(a[2]);
      }
      const float sq = rs * (0.18898223650461362f * 1.4426950408889634f);
      ln_pack(L, a[2], M_CSQ + 256, h, -mean * sq, sq, 0.f, qf[0], qf[1]);
  }
  v4u w2f[28][2];

  auto body = [&](auto LC) __attribute__((always_inline)) {
    constexpr int layer = decltype(LC)::value;
    FRESH();
    float mean, var;
    f32x16 sc[9];
    if (layer == 0) fkv = __hip_atomic_load(flag_line(ws, 0, E_KV + w, wg) + (lane & 1), RLX);
    {
      if (layer == 1) {
      f32x16 a[3];
      const unsigned tq = tr_base(lane, 0, true, j & 1);
      v2u st[7];
#pragma unroll
      for (int i = 0; i < 7; ++i) st[i] = LD8(W_X + TT * XB + X_ST + (7 * h + i) * 256 + n * 8);
      SB();
      v4u xg[28];
      kregs<2, true>(L, xg, tq, a, rws, W_X + TT * XB + lane * 16, [](int) {});
      row_stats(st, mean, var);
      const float rs = __builtin_amdgcn_rsqf(var + 1e-5f);
      asm volatile("s_nop 7" : "+v"(a[0]), "+v"(a[1]));
      const int kvoff = W_KV + 1 * KV_L + (j * 64 + TT) * 4096 + lane * 16;
      v4u v0, v1;
      ln_pack(L, a[0], M_CSQ, h, -mean * rs, rs, 0.f, kf[0], kf[1]);
      ST16(kf[0], kvoff); ST16(kf[1], kvoff + 1024);
      ln_pack(L, a[1], M_CSQ + 128, h, -mean * rs, rs, 1.f, v0, v1);
      ST16(v0, kvoff + 2048); ST16(v1, kvoff + 3072);
      if (w < 3) put_k(L, 8 + w, lane, kf[0], kf[1]);
      put_v(L, 8 + w, lane, v0, v1);
      kregs<1, false>(L, xg, tq + 2 * U, a + 2, rws, 0u, [&](int s) {
        if (s == 12) {
          VMWAIT(0);
          if (lane < 2 && c + 1 + lane < 16) __hip_atomic_store(flag_line(ws, 1, E_KV + (lane ? 0 : 2) + (w >> 1), wg + 16 * (1 + lane)) + (w & 1), epoch, RLX);
        }
      });
      fkv = __hip_atomic_load(flag_line(ws, 1, E_KV + w, wg) + (lane & 1), RLX);
      MFMA_DONE(a[2]);
      const float sq = rs * (0.18898223650461362f * 1.4426950408889634f);
      ln_pack(L, a[2], M_CSQ + 256, h, -mean * sq, sq, 0.f, qf[0], qf[1]);
      }
      if (4 * c - 8 + 2 * w >= 0) CHECK(fkv, flag_line(ws, layer, E_KV + w, wg) + (lane & 1));
    }
    FRESH();
    unsigned tp0 = tr_base(lane, 0, false, 0);
    {
      v4u hk[2][4];
      const int ho = 4 * c - 8 + 2 * w >= 0 ? W_KV + layer * KV_L + (j * 64 + 4 * c - 8 + 2 * w) * 4096 : W_END;
#pragma unroll
      for (int hh = 0; hh < 2; ++hh)
#pragma unroll
        for (int e = 0; e < 4; ++e) hk[hh][e] = __builtin_amdgcn_raw_buffer_load_b128(rws, lane * 16 + e * 1024, ho + hh * 4096, 16);
      SB();
#pragma unroll
      for (int r = 0; r < 16; ++r) {
        const bool past = (r & 3) + 8 * (r >> 2) + 4 * h > n;
        sc[0][r] = past ? 0.f : -1e30f;
        sc[8][r] = past ? -1e30f : 0.f;
      }
      asm volatile("s_nop 1" : "+v"(sc[0]), "+v"(sc[8]), "+v"(qf[0]), "+v"(qf[1]));
      mfma_vv(sc[8], kf[0], qf[0]); mfma_vv(sc[8], kf[1], qf[1]);
      float mx[4] = {-1e30f, -1e30f, -1e30f, -1e30f};
#define TMAX(T, a, b) do { _Pragma("unroll") for (int r_ = a; r_ < b; r_ += 2) mx[(r_ >> 1) & 3] = max3(mx[(r_ >> 1) & 3], T[r_], T[r_ + 1]); } while (0)
      MFMA_DONE(sc[8]);
      TMAX(sc[8], 0, 16);
      SB();
      if (layer == 0) {
        VMWAIT(8);
        asm volatile("" :: "v"(wo1.y));
        const unsigned ta = L_WO + w * 2048 + (4 * h + ((lane >> 2) & 3)) * 128 + (8 * ((lane >> 4) & 1) + 4 * ((lane >> 1) & 1) + 2 * (lane & 1)) * 8;
        const int o2 = (mlp && w + hf5 < 4) ? W_W2T + (jb * 112 + 7 * c + 4 * hf5 + w) * 2048 + lane * 16 : W_END;
        ST16(trf(L, ta, ta + 1024), o2);
        ST16(trf(L, ta + 8, ta + 1032), o2 + 1024);
        if (w == 3) *(LAS f32x2*)(L + M_B1 + lane * 8) = unpk(wo1.x);
        SB();
      }
#pragma unroll
      for (int hh = 0; hh < 2; ++hh) {
        put_k(L, 2 * w + hh, lane, hk[hh][0], hk[hh][1]);
        put_v(L, 2 * w + hh, lane, hk[hh][2], hk[hh][3]);
      }
      __syncthreads();
      if (layer == 0) {
        const rsrc_t ro = mkrs(wo, mlp ? DM * DM * 2 : 0);
#pragma unroll 1
        for (int i = 7 * w; i < 7 * w + 4; ++i) dma16(ro, (lane >> 2) * 896 + (lane & 3) * 16, i * 16 * 896 + j * 64, L_WO + i * 1024);
      }
      W1PIECE(21 * w); W1PIECE(21 * w + 1); W1PIECE(21 * w + 2);
      v4u kb[3][2];
      unsigned ka = L_K + w * 2048 + lane * 16;
      OPAQUE(ka);
#define KRD(i) do { kb[(i) % 3][0] = *(LAS v4u*)(L + ka + (i) * 2048); kb[(i) % 3][1] = *(LAS v4u*)(L + ka + (i) * 2048 + 1024); } while (0)
#define CLIP(i) do { if (__builtin_expect(TT - 8 + (i) < 0, 0)) { asm volatile("" ::: "memory"); _Pragma("unroll") for (int r_ = 0; r_ < 16; ++r_) sc[i][r_] = -1e30f; } } while (0)
      KRD(0); KRD(1); KRD(2);
      SB();
      mfma_vv(sc[0], kb[0][0], qf[0]); mfma_vv(sc[0], kb[0][1], qf[1]);
#pragma unroll
      for (int i = 1; i < 8; ++i) {
        mfma_v0(sc[i], kb[i % 3][0], qf[0]);
        asm volatile("s_nop 3" : "+v"(sc[i - 1]));
        CLIP(i - 1);
        TMAX(sc[i - 1], 0, 8);
        SB();
        mfma_vv(sc[i], kb[i % 3][1], qf[1]);
        if (i + 2 < 8) KRD(i + 2);
        TMAX(sc[i - 1], 8, 16);
        SB();
      }
      unsigned vb = L_V + w * 2048 + tp0;
      OPAQUE(vb);
      v4u vf[3];
      vf[0] = trf(L, vb, vb + 512);
      vf[1] = trf(L, vb + 1024, vb + 1536);
      if (layer == 0) {
        const rsrc_t ro = mkrs(wo, mlp ? DM * DM * 2 : 0);
#pragma unroll 1
        for (int i = 7 * w + 4; i < 7 * w + 7; ++i) dma16(ro, (lane >> 2) * 896 + (lane & 3) * 16, i * 16 * 896 + j * 64, L_WO + i * 1024);
      }
      MFMA_DONE(sc[7]);
      CLIP(7);
      TMAX(sc[7], 0, 16);
      float m = max3(max3(mx[0], mx[1], mx[2]), mx[3], mx[3]);
      m = max3(m, xh(m), m);
      const f32x2 nm = {-m, -m};
      f32x16 o;
      f32x2 e[4];
#pragma unroll
      for (int d = 0; d < 4; ++d) e[d] = f32x2{sc[0][2 * d], sc[0][2 * d + 1]} + nm;
#pragma unroll
      for (int st = 0; st < 18; ++st) {
        v4u pf;
        SB();
#pragma unroll
        for (int d = 0; d < 4; ++d) { e[d][0] = __builtin_amdgcn_exp2f(e[d][0]); e[d][1] = __builtin_amdgcn_exp2f(e[d][1]); }
        SB();
#pragma unroll
        for (int d = 0; d < 4; ++d) pf[d] = pk(e[d]);
        SB();
        if (st + 2 < 18) vf[(st + 2) % 3] = trf(L, vb + (st + 2) * 1024, vb + (st + 2) * 1024 + 512);
        else asm volatile("s_nop 1" : "+v"(pf));
        if (st < 12) { W1PIECE(21 * w + 3 + 3 * (st >> 1) + 2 * (st & 1)); if (!(st & 1)) W1PIECE(21 * w + 4 + 3 * (st >> 1)); }
        if (st + 1 < 18) {
#pragma unroll
          for (int d = 0; d < 4; ++d) e[d] = f32x2{sc[(st + 1) >> 1][8 * ((st + 1) & 1) + 2 * d], sc[(st + 1) >> 1][8 * ((st + 1) & 1) + 2 * d + 1]} + nm;
        }
        SB();
        if (st == 0) mfma_v0(o, vf[0], pf); else mfma_vv(o, vf[st % 3], pf);
        SB();
      }
      MFMA_DONE(o);
      const float other = xh(o[12]);
      const float inv = __builtin_amdgcn_rcpf(h ? o[12] : other);
      unsigned op[8];
#pragma unroll
      for (int e = 0; e < 8; ++e) op[e] = pk(f32x2{o[2 * e], o[2 * e + 1]} * inv);
      if (h == 1) { op[6] = 0u; op[7] = 0u; }
      ST16((v4u{op[0], op[1], op[2], op[3]}), W_O + TT * XB + j * 2048 + lane * 16);
      ST16((v4u{op[4], op[5], op[6], op[7]}), W_O + TT * XB + j * 2048 + 1024 + lane * 16);
      VMWAIT(0);
      __syncthreads();
      if (w == 0 && lane < 14) __hip_atomic_store(flag_line(ws, layer, E_O, c * 16 + lane) + j, epoch, RLX);
      if (layer == 0 && w == 1 && lane < 48 && (mlp || lane >= 32))
        __hip_atomic_store(lane < 32 ? flag_line(ws, 0, E_W2, (lane >> 1) * 16 + jb + 7 * (lane & 1)) + 2 * c + hf5 : flag_line(ws, 0, E_WQ, (lane & 15) * 16 + j) + c, epoch, RLX);
    }
    if (mlp) {
      FRESH();
#pragma unroll 1
      for (int i = 84 + 7 * w; i < 91 + 7 * w; ++i) W1PIECE(i);
      v2u res[4];
#pragma unroll
      for (int q = 0; q < 4; ++q)
        res[q] = layer == 0 ? res0[q] : LD8(W_X + TT * XB + 2 * j * 1024 + n * 16 + 8 * h + (q >> 1) * 1024 + (q & 1) * 512);
      f32x16 ca = zero16();
      if (layer == 0) colsum<4096, 2048>(L, tr_w1(lane, w), 3, ca);
      if (w == 0) poll(flag_line(ws, layer, E_O, wg) + (lane & 15), epoch, dead);
      __syncthreads();
      FRESH();
      {
        f32x16 a[1];
        oloop(L, rws, W_O + TT * XB + lane * 16, tp0 + L_WO, a[0]);
        MFMA_DONE(a[0]);
        unsigned o[8];
#pragma unroll
        for (int q = 0; q < 4; ++q) {
          o[2 * q] = pk(f32x2{a[0][4 * q], a[0][4 * q + 1]} + unpk(res[q].x));
          o[2 * q + 1] = pk(f32x2{a[0][4 * q + 2], a[0][4 * q + 3]} + unpk(res[q].y));
        }
        const int xo = W_X1 + TT * XB;
        ST16((v4u{o[0], o[1], o[2], o[3]}), xo + 2 * j * 1024 + lane * 16);
        ST16((v4u{o[4], o[5], o[6], o[7]}), xo + 2 * j * 1024 + 1024 + lane * 16);
        float s = 0.f, q2 = 0.f;
#pragma unroll
        for (int e = 0; e < 8; ++e) { s = dot2(o[e], ONES, s); q2 = dot2(o[e], o[e], q2); }
        s += xh(s); q2 += xh(q2);
        ST8((v2u{__builtin_bit_cast(unsigned, s), __builtin_bit_cast(unsigned, q2)}), h ? W_END : xo + X_ST + j * 256 + n * 8);
      }
      VMWAIT(0);
      __syncthreads();
      if (w == 0 && lane < 14) __hip_atomic_store(flag_line(ws, layer, E_X1, c * 16 + lane) + j, epoch, RLX);
      if (layer == 0) {
        colsum<4096, 2048>(L, tr_w1(lane, w) + 21 * 4096, 1, ca);
        colsum_put(L, ca, M_CS1 + w * 128, lane);
      }
      if (w == 0) poll((layer == 0 && lane >= 16) ? flag_line(ws, 0, E_W2, wg) + (lane - 16) : flag_line(ws, layer, E_X1, wg) + (lane < 14 ? lane : 0), epoch, dead);
      __syncthreads();
      FRESH();
      {
        v2u st[7];
#pragma unroll
        for (int i = 0; i < 7; ++i)
          asm volatile("buffer_load_dwordx2 %0, %1, %2, 0 offen offset:%3 sc1" : "=v"(st[i]) : "v"(W_X1 + TT * XB + X_ST + 7 * h * 256 + n * 8), "s"(rws), "i"(i * 256) : "memory");
        SB();
        f32x16 a[4];
        unsigned b4[4] = {tr_w1(lane, 0), tr_w1(lane, 1), tr_w1(lane, 2), tr_w1(lane, 3)};
        {
          v4u xr[7], wb[4];
          const unsigned xv = W_X1 + TT * XB + lane * 16;
          xfirst<0>(xr, rws, xv);
#pragma unroll
          for (int q = 0; q < 4; ++q) { OPAQUE(b4[q]); wb[q] = trf(L, b4[q], b4[q] + 2048); }
          k4step<layer == 0, 0>(L, xr, rws, xv, b4, wb, a, w2f, W2V);
        }
        asm volatile("" : "+v"(st[0]), "+v"(st[1]), "+v"(st[2]), "+v"(st[3]), "+v"(st[4]), "+v"(st[5]), "+v"(st[6]));
        if (layer == 0) w2range<42, 56>(w2f, rws, W2V);
        f32x4 cs[4], bb1[4];
#pragma unroll
        for (int q = 0; q < 4; ++q) { cs[q] = *(LAS f32x4*)(L + M_CS1 + q * 32 + h * 16); bb1[q] = *(LAS f32x4*)(L + M_B1 + q * 32 + h * 16); }
        row_stats(st, mean, var);
        float rs = __builtin_amdgcn_rsqf(var + 1e-5f);
        rs = rs * __builtin_amdgcn_rsqf(var * rs * rs + 1e-5f);
        const float nmr = -mean * rs;
        asm volatile("s_nop 7" : "+v"(a[0]), "+v"(a[1]), "+v"(a[2]), "+v"(a[3]));
#pragma unroll
        for (int cc = 0; cc < 4; ++cc) {
          unsigned o[8];
          f32x2 v[8], z[8];
#pragma unroll
          for (int e = 0; e < 8; ++e) z[e] = f32x2{cs[e >> 1][2 * (e & 1)], cs[e >> 1][2 * (e & 1) + 1]} * nmr + f32x2{bb1[e >> 1][2 * (e & 1)], bb1[e >> 1][2 * (e & 1) + 1]};
          SB();
          if (cc < 3) {
#pragma unroll
            for (int q = 0; q < 4; ++q) { cs[q] = *(LAS f32x4*)(L + M_CS1 + (cc + 1) * 128 + q * 32 + h * 16); bb1[q] = *(LAS f32x4*)(L + M_B1 + (cc + 1) * 128 + q * 32 + h * 16); }
          }
#pragma unroll
          for (int e = 0; e < 8; ++e) v[e] = f32x2{a[cc][2 * e], a[cc][2 * e + 1]} * rs + z[e];
          SB();
#pragma unroll
          for (int e = 0; e < 8; ++e) z[e] = v[e] * v[e];
          SB();
#pragma unroll
          for (int e = 0; e < 8; ++e) z[e] = z[e] * -0.10294324f - 2.3022082f;
          SB();
#pragma unroll
          for (int e = 0; e < 8; ++e) z[e] *= v[e];
          SB();
#pragma unroll
          for (int e = 0; e < 8; ++e) { z[e][0] = __builtin_amdgcn_exp2f(z[e][0]); z[e][1] = __builtin_amdgcn_exp2f(z[e][1]); }
          SB();
#pragma unroll
          for (int e = 0; e < 8; ++e) z[e] += 1.0f;
          SB();
#pragma unroll
          for (int e = 0; e < 8; ++e) { z[e][0] = __builtin_amdgcn_rcpf(z[e][0]); z[e][1] = __builtin_amdgcn_rcpf(z[e][1]); }
          SB();
#pragma unroll
          for (int e = 0; e < 8; ++e) v[e] *= z[e];
          SB();
#pragma unroll
          for (int e = 0; e < 8; ++e) o[e] = pk(v[e]);
          const int ho = W_H + TT * HB + (8 * j + 2 * cc) * 1024 + lane * 16;
          ST16((v4u{o[0], o[1], o[2], o[3]}), ho);
          ST16((v4u{o[4], o[5], o[6], o[7]}), ho + 1024);
          SB();
          if (cc == 2) {
            VMWAIT(2);
            __builtin_amdgcn_s_barrier();
            if (w == 0 && lane < 14) __hip_atomic_store(flag_line(ws, layer, E_H0, c * 16 + lane) + j, epoch, RLX);
          }
        }
      }
      GAS u64* fp0 = flag_line(ws, layer, E_H0, wg) + (lane < 14 ? lane : 0);
      const u64 f0 = __hip_atomic_load(fp0, RLX);
      VMWAIT(0);
      __syncthreads();
      if (w == 0 && lane < 14) __hip_atomic_store(flag_line(ws, layer, E_H1, c * 16 + lane) + j, epoch, RLX);
      FRESH();
      const int T5 = 4 * c + 2 * hf5, k5 = 4 * jb + w;
      v2u r5[2][2];
      v4u bb2;
      CHECK(f0, fp0);
      {
        f32x16 acc[2][2];
        v4u hf[2 * (PW + 1)];
        const unsigned hv = W_H + T5 * HB + w * 1024 + lane * 16;
        auto ldh = [&](int i) {
          const int so = (i < 14 ? 8 * i : 8 * (i - 14) + 4) * 1024;
          hf[2 * (i % (PW + 1))] = __builtin_amdgcn_raw_buffer_load_b128(rws, hv, so, 16);
          hf[2 * (i % (PW + 1)) + 1] = __builtin_amdgcn_raw_buffer_load_b128(rws, hv, so + HB, 16);
        };
        GAS u64* fp1 = flag_line(ws, layer, E_H1, wg) + (lane < 14 ? lane : 0);
        u64 f1;
#pragma unroll
        for (int i = 0; i < PW; ++i) ldh(i);
        SB();
#pragma unroll
        for (int i = 0; i < 28; ++i) {
#pragma unroll
          for (int m = 0; m < 2; ++m) {
#pragma unroll
            for (int x = 0; x < 2; ++x) { if (i == 0) mfma_a0(acc[x][m], w2f[i][x], hf[m]); else mfma_av(acc[x][m], w2f[i][x], hf[2 * (i % (PW + 1)) + m]); }
            SB();
            if (m == 0 && i == 22) {
#pragma unroll
      for (int m = 0; m < 2; ++m)
#pragma unroll
        for (int e = 0; e < 2; ++e) r5[m][e] = LD8(W_X1 + (T5 + m) * XB + k5 * 1024 + e * 512 + n * 16 + 8 * h);
      bb2 = *(const v4u*)(b2 + k5 * 16 + 8 * h);
            }
            if (m == 0 && i == 10 - PW) f1 = __hip_atomic_load(fp1, RLX);
            if (m == 0 && i == 14 - PW) CHECK(f1, fp1);
            if (m == 0 && i + PW < 28) ldh(i + PW);
            if (m == 0 && i == 24 && layer == 0) WQ7(14 * w);
            SB();
          }
        }
        FRESH();
        asm volatile("s_nop 11" : "+v"(acc[0][0]), "+v"(acc[0][1]), "+v"(acc[1][0]), "+v"(acc[1][1]));
#pragma unroll
        for (int d = 0; d < 4; ++d)
#pragma unroll
          for (int x = 0; x < 2; ++x)
#pragma unroll
            for (int m = 0; m < 2; ++m)
              *(LAS f32x4*)(L + L_RED + w * 4096 + lane * 16 + d * 16384 + (x * 2 + m) * 1024) = f32x4{acc[x][m][4 * d], acc[x][m][4 * d + 1], acc[x][m][4 * d + 2], acc[x][m][4 * d + 3]};
        __syncthreads();
        if (layer == 0) WQ7(14 * w + 7);
        f32x4 p[2][2];
#pragma unroll
        for (int s = 0; s < 4; ++s)
#pragma unroll
          for (int x = 0; x < 2; ++x)
#pragma unroll
            for (int m = 0; m < 2; ++m) { const f32x4 t = *(LAS f32x4*)(L + L_RED + w * 16384 + lane * 16 + s * 4096 + (x * 2 + m) * 1024); p[x][m] = s == 0 ? t : p[x][m] + t; }
#pragma unroll
        for (int m = 0; m < 2; ++m) {
          const unsigned rr[4] = {r5[m][0].x, r5[m][0].y, r5[m][1].x, r5[m][1].y};
          v4u o;
#pragma unroll
          for (int d = 0; d < 4; ++d) o[d] = pk(f32x2{p[d >> 1][m][2 * (d & 1)], p[d >> 1][m][2 * (d & 1) + 1]} + unpk(rr[d]) + unpk(bb2[d]));
          if (layer == 0) {
            ST16(o, W_X + (4 * c + 2 * hf5 + m) * XB + (4 * jb + w) * 1024 + lane * 16);
            float s = 0.f, q2 = 0.f;
#pragma unroll
            for (int e = 0; e < 4; ++e) { s = dot2(o[e], ONES, s); q2 = dot2(o[e], o[e], q2); }
            *(LAS f32x2*)(L + L_RST + ((m * 8 + w * 2 + h) * 32 + n) * 8) = f32x2{s, q2};
          } else {
            *(v4u*)(out + (32 * (4 * c + 2 * hf5 + m) + n) * 448 + (4 * jb + w) * 16 + 8 * h) = o;
          }
        }
      }
    } else if (layer == 1 && wg == 15 && w == 0) {
      bool ok = false;
      for (int spins = 0; spins < SPIN_CAP && !ok; ++spins) {
        bool a = true;
#pragma unroll
        for (int i = 0; i < 4; ++i) a &= __hip_atomic_load((GAS u64*)(ws + W_STARTED) + lane + 64 * i, RLX) == epoch;
        ok = __builtin_amdgcn_ballot_w64(!a) == 0;
      }
      if (lane == 0) __hip_atomic_store((GAS u64*)(ws + W_E), epoch, RLX);
    }
    if (layer == 0) {
      FRESH();
      __syncthreads();
      if (mlp && w < 2) {
        f32x2 t = {0.f, 0.f};
#pragma unroll
        for (int i = 0; i < 4; ++i) t += *(LAS f32x2*)(L + L_RST + ((h * 8 + 4 * w + i) * 32 + n) * 8);
        ST8(__builtin_bit_cast(v2u, t), W_X + (4 * c + 2 * hf5 + h) * XB + X_ST + (2 * jb + w) * 256 + n * 8);
      }
      VMWAIT(0);
      __syncthreads();
      if (w == 0 && mlp && lane < 16) __hip_atomic_store(flag_line(ws, 1, E_X, c * 16 + lane) + j, epoch, RLX);
      if (!mlp) {
        poll(flag_line(ws, 0, E_WQ, wg) + (lane & 15), epoch, dead);
        WQ7(14 * w); WQ7(14 * w + 7);
      }
      FRESH();
      WQ7(56 + 7 * w);
      if (w == 0) poll(flag_line(ws, 1, E_X, wg) + (lane < 14 ? lane : 0), epoch, dead);
      VMWAIT(0);
      __syncthreads();
    }
  };
  body(IC<0>{});
  body(IC<1>{});
}
}

extern "C" void kernel_launch(void* const* d_in, const int* in_sizes, int n_in, void* d_out, int out_size, void* d_ws, size_t ws_size, hipStream_t stream) {
  (void)hipFuncSetAttribute((const void*)mk::fused, hipFuncAttributeMaxDynamicSharedMemorySize, mk::L_TOTAL);
  mk::fused<<<256, 256, mk::L_TOTAL, stream>>>((const unsigned short*)d_in[0], (const unsigned short*)d_in[1], (const unsigned short*)d_in[2],
      (const unsigned short*)d_in[4], (const unsigned short*)d_in[5], (const unsigned short*)d_in[6], (const unsigned short*)d_in[7],
      (unsigned short*)d_out, (unsigned char*)d_ws);
}
```
